```python
import torch

def _softmax(x, axis=-1):
    return torch.softmax(x, dim=axis)

def _gelu(x):
    return torch.nn.functional.gelu(x, approximate='tanh')

def _silu(x):
    return torch.nn.functional.silu(x)

def _sigmoid(x):
    return torch.sigmoid(x)

def _layernorm(x, axis=-1, eps=1e-05):
    m = x.mean(dim=axis, keepdim=True)
    v = x.var(dim=axis, keepdim=True, unbiased=False)
    return (x - m) / torch.sqrt(v + eps)

def _scatter_add(tgt, idx, src, axis=0):
    return tgt.clone().index_add_(axis, idx, src)

def _segment_sum(src, seg, n):
    out = torch.zeros((n,) + src.shape[1:], dtype=src.dtype)
    return out.index_add_(0, seg, src)

def _pad(x, pw):
    flat = tuple((v for pair in reversed(pw) for v in pair))
    return torch.nn.functional.pad(x, flat)
kernel_name = 'gated_mix__mha_block__memory_aug_recurrent_mem_tr__prompt'

def setup_inputs():

    _w = lambda a: (lambda t: t.to(torch.bfloat16) if t.is_floating_point() else t)(torch.as_tensor(a))

    def _A_setup():
        B, S, D, H = (1, 2048, 640, 32)
        return {'x': _w((lambda *s: torch.randn(*s, dtype=torch.bfloat16) * 0.05)(B, S, D)), 'wqkv': _w((lambda *s: torch.randn(*s, dtype=torch.bfloat16) * 0.8)(D, 3 * D)), 'wo': _w((lambda *s: torch.randn(*s, dtype=torch.bfloat16) * 0.05)(D, D))}
    _A_in = _A_setup()

    def _B_setup():
        B, S, Dm = (1, 2048, 640)
        H, Dh = (32, 128)
        M, seg = (8, 32)
        seg = min(seg, S)
        S = S // seg * seg
        return {'x': _w((lambda *s: torch.randn(*s, dtype=torch.bfloat16) * 0.05)(B, S, Dm)), 'mem0': _w((lambda *s: torch.randn(*s, dtype=torch.bfloat16) * 0.8)(B, M, Dm)), 'Wq': _w((lambda *s: torch.randn(*s, dtype=torch.bfloat16) * 3.2)(Dm, H, Dh) * Dm ** (-0.5)), 'Wk': _w((lambda *s: torch.randn(*s, dtype=torch.bfloat16) * 3.2)(Dm, H, Dh) * Dm ** (-0.5)), 'Wv': _w((lambda *s: torch.randn(*s, dtype=torch.bfloat16) * 0.8)(Dm, H, Dh) * Dm ** (-0.5)), 'Wo': _w((lambda *s: torch.randn(*s, dtype=torch.bfloat16) * 0.8)(H, Dh, Dm) * (H * Dh) ** (-0.5)), 'seg_len': seg}
    _B_in = _B_setup()
    return {'A_x': _A_in['x'], 'A_wqkv': _A_in['wqkv'], 'A_wo': _A_in['wo'], 'B_x': _B_in['x'], 'B_mem0': _B_in['mem0'], 'B_Wq': _B_in['Wq'], 'B_Wk': _B_in['Wk'], 'B_Wv': _B_in['Wv'], 'B_Wo': _B_in['Wo'], 'B_seg_len': _B_in['seg_len'], 'gate_w': _w((lambda *s: torch.randn(*s, dtype=torch.bfloat16) * 0.05)(640) * 16.0)}

def reference(A_x, A_wqkv, A_wo, B_x, B_mem0, B_Wq, B_Wk, B_Wv, B_Wo, B_seg_len, gate_w):

    def _A_ref(x, wqkv, wo):
        B, S, D = x.shape
        H = 32
        Dh = D // H
        qkv = (lambda x, *d: x.permute(*d))((x @ wqkv).reshape(B, S, 3, H, Dh), 2, 0, 3, 1, 4)
        q, k, v = (qkv[0], qkv[1], qkv[2])
        scores = torch.einsum('bhqd,bhkd->bhqk', q, k) * Dh ** (-0.5)
        p = _softmax(scores, axis=-1)
        o = torch.einsum('bhqk,bhkd->bhqd', p, v)
        return (lambda x, *d: x.permute(*d))(o, 0, 2, 1, 3).reshape(B, S, D) @ wo

    def _B_ref(x, mem0, Wq, Wk, Wv, Wo, seg_len):
        B, S, Dm = x.shape
        H, Dh = (Wq.shape[1], Wq.shape[2])
        M = mem0.shape[1]
        scale = Dh ** (-0.5)
        n_seg = S // seg_len
        L = M + seg_len
        idx = torch.arange(L)
        mask = (idx[:, None] < M) | (idx[:, None] >= idx[None, :])
        mem = mem0
        outs = []
        for s in range(n_seg):
            xs = x[:, s * seg_len:(s + 1) * seg_len, :].clone()
            h = torch.concatenate([mem, xs], axis=1)
            q = torch.einsum('btd,dhe->bhte', h, Wq)
            k = torch.einsum('btd,dhe->bhte', h, Wk)
            v = torch.einsum('btd,dhe->bhte', h, Wv)
            scores = torch.einsum('bhqe,bhke->bhqk', q, k) * scale
            scores = torch.where(mask[None, None], scores, -1000000000.0)
            p = _softmax(scores, axis=-1)
            o = torch.einsum('bhqk,bhke->bhqe', p, v)
            o = torch.einsum('bhte,hed->btd', o, Wo)
            mem = o[:, :M, :].clone()
            outs.append(o[:, M:, :])
        return torch.concatenate(outs, axis=1)
    x = A_x
    g = _sigmoid((x * gate_w).sum(-1, keepdims=True))
    a = _A_ref(x, A_wqkv, A_wo)
    b = _B_ref(x, B_mem0, B_Wq, B_Wk, B_Wv, B_Wo, B_seg_len)
    return g * a + (1 - g) * b
```

```cpp
#include <hip/hip_runtime.h>
#include <hip/hip_bf16.h>
#include <stdint.h>

typedef unsigned short u16;
typedef unsigned int u32;
typedef unsigned long long u64;
typedef __bf16 bf16x2 __attribute__((ext_vector_type(2)));
typedef __bf16 bf16x8 __attribute__((ext_vector_type(8)));
typedef __bf16 bf16x16 __attribute__((ext_vector_type(16)));
typedef float f32x2 __attribute__((ext_vector_type(2)));
typedef float f32x4 __attribute__((ext_vector_type(4)));
typedef float f32x16 __attribute__((ext_vector_type(16)));
typedef u32 u32x2 __attribute__((ext_vector_type(2)));
typedef u32 u32x4 __attribute__((ext_vector_type(4)));
typedef u32 u32x8 __attribute__((ext_vector_type(8)));
#define GAS __attribute__((address_space(1)))
#define LAS __attribute__((address_space(3)))
typedef GAS u32 gu32;
typedef LAS unsigned char lchar;
#define RLX_AGENT __ATOMIC_RELAXED, __HIP_MEMORY_SCOPE_AGENT
#define DEV __device__ __forceinline__

constexpr int NSEG = 64;
constexpr u32 SENT = 0xFFFFFFFFu;
constexpr u32 MB = 1u << 20;
constexpr u32 OFF_FLAG = 3 * MB;
constexpr u32 OFF_MLOG = OFF_FLAG + 65536;
constexpr u32 ARM_BYTES = OFF_MLOG + NSEG * 10240;
constexpr u32 OFF_U = 4 * MB;
constexpr u32 OFF_YL = OFF_U + 80 * MB;
constexpr u32 OFF_KX = OFF_YL + 20 * MB;
constexpr u32 OFF_PT = OFF_KX + 16 * MB;
constexpr u32 OFF_Q0 = OFF_PT + 5 * MB;
constexpr u32 OFF_Q1 = OFF_Q0 + 2 * MB;
constexpr u32 OFF_KV = OFF_Q1 + 1 * MB;
constexpr u32 OFF_OA = OFF_KV + 5 * MB;
constexpr u32 OFF_WOF = OFF_OA + 3 * MB;
constexpr u32 OFF_G = OFF_WOF + 1 * MB;
constexpr u32 OFF_DBG = OFF_G + 1 * MB;
constexpr u32 WS_BYTES = OFF_DBG + 1 * MB;
constexpr int F_POL = 0, F_ABORT = 1, F_BAR1 = 2, F_BAR2 = 3, F_KX = 4, F_U = 36, F_SEG = 68;
constexpr int SEG_TARGET = 32 * 4 + 32 * 2 + 32 * 4;
constexpr int NWRK = 88;
constexpr int KQ_LEAD = 2;
constexpr int LDS_BYTES = 156 * 1024;
constexpr u64 TMO_TICKS = 30000000ull;
constexpr float LOG2E = 1.4426950408889634f;

struct Params {
    const u16 *x, *wqkv, *wo, *mem0, *Wq, *Wk, *Wv, *Wo, *gw;
    u16* out;
    unsigned char* ws;
};

typedef __amdgpu_buffer_rsrc_t rsrc_t;
DEV u64 rt() { return __builtin_amdgcn_s_memrealtime(); }
DEV u32 pk(float a, float b) { f32x2 v = {a, b}; return __builtin_bit_cast(u32, __builtin_convertvector(v, bf16x2)); }
DEV u32 guard(u32 v) { return v < 0xFFFFFFFEu ? v : 0xFFFFFFFEu; }
DEV float fmx(float a, float b) { return __builtin_elementwise_maximum(a, b); }
DEV u32 umx3(u32 a, u32 b, u32 c) { return max(max(a, b), c); }
DEV u32 umx4(u32x4 v) { return max(umx3(v.x, v.y, v.z), v.w); }
DEV float bf_lo(u32 v) { return __uint_as_float(v << 16); }
DEV float bf_hi(u32 v) { return __uint_as_float(v & 0xFFFF0000u); }
DEV u32x4 lo4(const u32x8& v) { return __builtin_shufflevector(v, v, 0, 1, 2, 3); }
DEV u32x4 hi4(const u32x8& v) { return __builtin_shufflevector(v, v, 4, 5, 6, 7); }
DEV u32x8 cat8(u32x4 a, u32x4 b) { return __builtin_shufflevector(a, b, 0, 1, 2, 3, 4, 5, 6, 7); }
DEV f32x4 mma16(u32x4 a, u32x4 b, f32x4 c) {
    return __builtin_amdgcn_mfma_f32_16x16x32_bf16(__builtin_bit_cast(bf16x8, a), __builtin_bit_cast(bf16x8, b), c, 0, 0, 0); }
DEV f32x16 mma32(u32x4 a, u32x4 b, f32x16 c) {
    return __builtin_amdgcn_mfma_f32_32x32x16_bf16(__builtin_bit_cast(bf16x8, a), __builtin_bit_cast(bf16x8, b), c, 0, 0, 0); }
DEV f32x4 smma(u32x4 a, const u32x8& b, f32x4 c, int idx) {
    return __builtin_amdgcn_smfmac_f32_16x16x64_bf16(__builtin_bit_cast(bf16x8, a), __builtin_bit_cast(bf16x16, b), c, idx, 0, 0); }
DEV float swap_add(float a, float b) {
    auto r = __builtin_amdgcn_permlane32_swap(__float_as_uint(a), __float_as_uint(b), false, false);
    return __uint_as_float(r[0]) + __uint_as_float(r[1]); }
DEV float ror8(float v) { return __uint_as_float(__builtin_amdgcn_update_dpp(0, __float_as_uint(v), 0x128, 0xf, 0xf, false)); }
DEV float max_x16(float v) { auto r = __builtin_amdgcn_permlane16_swap(__float_as_uint(v), __float_as_uint(v), false, false); return fmx(__uint_as_float(r[0]), __uint_as_float(r[1])); }
DEV float max_x32(float v) { auto r = __builtin_amdgcn_permlane32_swap(__float_as_uint(v), __float_as_uint(v), false, false); return fmx(__uint_as_float(r[0]), __uint_as_float(r[1])); }
DEV float add_x16(float v) { auto r = __builtin_amdgcn_permlane16_swap(__float_as_uint(v), __float_as_uint(v), false, false); return __uint_as_float(r[0]) + __uint_as_float(r[1]); }
DEV float add_x32(float v) { auto r = __builtin_amdgcn_permlane32_swap(__float_as_uint(v), __float_as_uint(v), false, false); return __uint_as_float(r[0]) + __uint_as_float(r[1]); }

DEV u32x4 ld4(rsrc_t rs, u32 vo, u32 so) { return __builtin_amdgcn_raw_buffer_load_b128(rs, vo, so, 16); }
DEV u32x2 ld2(rsrc_t rs, u32 vo, u32 so) { return __builtin_amdgcn_raw_buffer_load_b64(rs, vo, so, 16); }
DEV u32 ld1(rsrc_t rs, u32 vo, u32 so) { return __builtin_amdgcn_raw_buffer_load_b32(rs, vo, so, 16); }
DEV void st4(u32x4 v, rsrc_t rs, u32 vo, u32 so) { __builtin_amdgcn_raw_buffer_store_b128(v, rs, vo + so, 0, 16); }
DEV void st2(u32x2 v, rsrc_t rs, u32 vo, u32 so) { __builtin_amdgcn_raw_buffer_store_b64(v, rs, vo, so, 16); }
DEV void st1(u32 v, rsrc_t rs, u32 vo, u32 so) { __builtin_amdgcn_raw_buffer_store_b32(v, rs, vo, so, 16); }
DEV rsrc_t mk_rsrc(const void* p, u32 bytes) { return __builtin_amdgcn_make_buffer_rsrc((void*)p, 0, (int)bytes, 0x00020000); }
DEV u32x4 pld4(rsrc_t rs, u32 vo, u32 so) { return __builtin_amdgcn_raw_buffer_load_b128(rs, vo, so, 0); }
DEV u32x2 pld2(rsrc_t rs, u32 vo, u32 so) { return __builtin_amdgcn_raw_buffer_load_b64(rs, vo, so, 0); }
DEV u32 pld1(rsrc_t rs, u32 vo, u32 so) { return __builtin_amdgcn_raw_buffer_load_b32(rs, vo, so, 0); }
DEV void pst2(u32x2 v, rsrc_t rs, u32 vo, u32 so) { __builtin_amdgcn_raw_buffer_store_b64(v, rs, vo, so, 0); }
DEV u32x4 lds4(lchar* p) { return *(LAS u32x4*)p; }
DEV u32x2 lds2(lchar* p) { return *(LAS u32x2*)p; }
#define BAR() do { asm volatile("s_waitcnt lgkmcnt(0)" ::: "memory"); __builtin_amdgcn_s_barrier(); asm volatile("" ::: "memory"); } while (0)
#define DRAIN() asm volatile("s_waitcnt vmcnt(0)" ::: "memory")
#define FENCE() asm volatile("" ::: "memory")

struct Ctx {
    rsrc_t rs;
    gu32* fl;
    u64 t0;
    int tid, wave, lane, n, g;
    lchar* lds;
};
DEV gu32* flag(const Ctx& c, int i) { return c.fl + i * 64; }
DEV bool give_up(const Ctx& c, int& sp) {
    if ((++sp & 255) != 0) return false;
    if (__builtin_amdgcn_readfirstlane(__hip_atomic_load(flag(c, F_ABORT), RLX_AGENT)) != (int)SENT) return true;
    if (rt() - c.t0 > TMO_TICKS) { __hip_atomic_store(flag(c, F_ABORT), 1u, RLX_AGENT); return true; }
    return false;
}
DEV bool wait_ge(const Ctx& c, int fi, int want) {
    int sp = 0;
    for (;;) {
        FENCE();
        if (__builtin_amdgcn_readfirstlane(__hip_atomic_load(flag(c, fi), RLX_AGENT)) >= want) return true;
        __builtin_amdgcn_s_sleep(2);
        if (give_up(c, sp)) return false;
    }
}
DEV u32 slot_base(int slot, u32 pol) { return (u32)slot * MB + (((pol >> (slot >> 1)) & 1u) << 13); }

#define STAMP(role_id, s, k) do { } while (0)

template <int NK64, int LDW>
DEV void load_w(rsrc_t rw, u32 base, int n, int g, u32x8* w0, u32x8* w1) {
    const u32 vo = (u32)(8 * g * LDW + 2 * n) * 2;
#pragma unroll
    for (int k = 0; k < NK64; ++k) {
        u32 d[16];
#pragma unroll
        for (int j = 0; j < 16; ++j) d[j] = pld1(rw, vo, base + (u32)(64 * k + 32 * (j >> 3) + (j & 7)) * (LDW * 2));
#pragma unroll
        for (int j = 0; j < 8; ++j) {
            w0[k][j] = __builtin_amdgcn_perm(d[2 * j + 1], d[2 * j], 0x05040100);
            w1[k][j] = __builtin_amdgcn_perm(d[2 * j + 1], d[2 * j], 0x07060302);
        }
    }
}

DEV bool poll_m(const Ctx& c, u32 vo, u32 so, lchar* dst) {
    u32x4 v; u32 u; int sp = 0;
    for (;;) {
        FENCE();
        v = ld4(c.rs, vo + c.lane * 16, so);
        u = ld1(c.rs, vo + 1024 + c.lane * 4, so);
        if (__builtin_amdgcn_ballot_w64(umx3(umx3(v.x, v.y, v.z), v.w, u) == SENT) == 0) break;
        if (give_up(c, sp)) return false;
    }
    *(LAS u32x4*)(dst + c.wave * 1280 + c.lane * 16) = v;
    *(LAS u32*)(dst + c.wave * 1280 + 1024 + c.lane * 4) = u;
    return true;
}
DEV void mem_proj(const Ctx& c, lchar* mimg, const u32x8* w0, const u32x8* w1, int idx, u32& oa, u32& ob) {
    f32x4 a0 = {0.f, 0.f, 0.f, 0.f}, a1 = a0;
#pragma unroll
    for (int t = 0; t < 10; ++t) {
        const u32x4 a = lds4(mimg + t * 1024 + c.lane * 16);
        a0 = smma(a, w0[t], a0, idx);
        a1 = smma(a, w1[t], a1, idx);
    }
    oa = pk(swap_add(a0[0], a0[2]), swap_add(a1[0], a1[2]));
    ob = pk(swap_add(a0[1], a0[3]), swap_add(a1[1], a1[3]));
}
DEV void x_load(const Ctx& c, const u16* x, int seg, u32x4 (&r)[5]) {
    const rsrc_t rx = mk_rsrc(x, 2048 * 640 * 2);
    const u32 vo = (u32)(c.n * 640 + 8 * c.g) * 2;
    seg = seg < NSEG ? seg : NSEG - 1;
#pragma unroll
    for (int k = 0; k < 5; ++k) {
        const int f = c.wave * 5 + k, tt = f / 20, ks = f % 20;
        r[k] = pld4(rx, vo, (u32)((32 * seg + 16 * tt) * 640 + 32 * ks) * 2);
    }
}
DEV void x_store(const Ctx& c, lchar* dst, const u32x4 (&r)[5]) {
#pragma unroll
    for (int k = 0; k < 5; ++k) *(LAS u32x4*)(dst + (c.wave * 5 + k) * 1024 + c.lane * 16) = r[k];
}
DEV void tok_proj(const Ctx& c, lchar* ximg, const u32x8* w0, const u32x8* w1, u32x4 (&o)[2]) {
    f32x4 acc[2][2];
#pragma unroll
    for (int i = 0; i < 4; ++i) acc[i >> 1][i & 1] = (f32x4){0.f, 0.f, 0.f, 0.f};
#pragma unroll
    for (int ks = 0; ks < 20; ++ks) {
        const u32x4 a0 = (ks & 1) ? hi4(w0[ks >> 1]) : lo4(w0[ks >> 1]);
        const u32x4 a1 = (ks & 1) ? hi4(w1[ks >> 1]) : lo4(w1[ks >> 1]);
#pragma unroll
        for (int tt = 0; tt < 2; ++tt) {
            const u32x4 b = lds4(ximg + (tt * 20 + ks) * 1024 + c.lane * 16);
            acc[tt][0] = mma16(a0, b, acc[tt][0]);
            acc[tt][1] = mma16(a1, b, acc[tt][1]);
        }
    }
#pragma unroll
    for (int tt = 0; tt < 2; ++tt)
#pragma unroll
        for (int r = 0; r < 4; ++r) o[tt][r] = pk(acc[tt][0][r], acc[tt][1][r]);
}

DEV void role_qk(const Params& P, const Ctx& c, int h, int rep) {
    const int w = c.wave, n = c.n, g = c.g, lane = c.lane;
    u32x8 w0[10], w1[10];
    load_w<10, 4096>(mk_rsrc(w < 4 ? P.Wq : P.Wk, 640 * 4096 * 2), (u32)(h * 128 + 32 * (w & 3)) * 2, n, g, w0, w1);
    if (!wait_ge(c, F_POL, 0)) return;
    const u32 pol = __builtin_amdgcn_readfirstlane(__hip_atomic_load(flag(c, F_POL), RLX_AGENT));
    lchar* mimg = c.lds;
    lchar* qs = c.lds + 10240;
    const int idx = n < 8 ? 0x4444 : 0xEEEE;
    const u32 m_vo = (u32)((2 * rep + (w >> 2)) * 16384 + (w & 3) * 1280);
    lchar* q_wr = qs + (w >> 2) * 2304 + (w & 3) * 576 + (n >> 2) * 144 + (4 * (g & 1) + 2 * (g >> 1)) * 16 + (n & 3) * 4;
    lchar* q_rd = qs + g * 144 + (n & 7) * 16;
    const int pr = (n & 1) | ((n & 2) << 1) | ((n & 4) >> 1);
    lchar* k_rd = qs + 2304 + g * 144 + pr * 16;
    const u32 px_vo = (u32)((8 + 3 * (w & 3) + (h >> 4)) * 16384 + ((h >> 1) & 7) * 1024 + ((2 * (h & 1) + (g >> 1)) * 16 + n) * 16 + (g & 1) * 8);
    const bool mval = g == (n >> 3);
    const u32 pm_vo = mval ? (u32)((8 + 3 * (w & 3) + 2) * 16384 + h * 128 + n * 8) : 0x80000000u;
    const float cs = 0.08838834764831845f * LOG2E;
    u32x4 kx[8];
    if (w < 4) {
        if (!wait_ge(c, F_KX + h, 0)) return;
#pragma unroll
        for (int i = 0; i < 8; ++i) kx[i] = ld4(c.rs, OFF_KX + (u32)(h * 8 + i) * 1024 + lane * 16, 0);
    }
    int slot = 0;
    for (int s = 0; s < NSEG - 1; ++s) {
        const u32 sb = slot_base(slot, pol);
        if (!poll_m(c, m_vo, sb, mimg)) return;
        BAR();
        STAMP(h, s, 0);
        u32 oa, ob;
        mem_proj(c, mimg, w0, w1, idx, oa, ob);
        *(LAS u32*)(q_wr) = oa;
        *(LAS u32*)(q_wr + 16) = ob;
        BAR();
        const int slot2 = slot == 0 ? 2 : slot - 1;
        if (w < 4) {
            f32x4 d0 = {0.f, 0.f, 0.f, 0.f}, d1 = d0, d2 = d0;
#pragma unroll
            for (int ks = 0; ks < 4; ++ks) {
                const u32x4 q = lds4(q_rd + ks * 576), km = lds4(k_rd + ks * 576);
                d0 = mma16(kx[ks], q, d0);
                d1 = mma16(kx[4 + ks], q, d1);
                d2 = mma16(km, q, d2);
            }
            float v[8];
#pragma unroll
            for (int r = 0; r < 4; ++r) { v[r] = n < 8 ? d0[r] : d1[r]; v[4 + r] = mval ? d2[r] : -INFINITY; }
            float mx = fmx(fmx(fmx(v[0], v[1]), fmx(v[2], v[3])), fmx(fmx(v[4], v[5]), fmx(v[6], v[7])));
            mx = fmx(mx, ror8(mx)); mx = max_x16(mx); mx = max_x32(mx);
            const float mc = -mx * cs;
            float sum = 0.f;
#pragma unroll
            for (int r = 0; r < 8; ++r) { v[r] = __builtin_amdgcn_exp2f(__builtin_fmaf(v[r], cs, mc)); }
            sum = ((v[0] + v[1]) + (v[2] + v[3])) + ((v[4] + v[5]) + (v[6] + v[7]));
            sum += ror8(sum); sum = add_x16(sum); sum = add_x32(sum);
            const float inv = __builtin_amdgcn_rcpf(sum);
            st2((u32x2){guard(pk(v[0] * inv, v[1] * inv)), guard(pk(v[2] * inv, v[3] * inv))}, c.rs, px_vo, sb);
            st2((u32x2){guard(pk(v[4] * inv, v[5] * inv)), guard(pk(v[6] * inv, v[7] * inv))}, c.rs, pm_vo, sb);
            STAMP(h, s, 1);
            FENCE();
            const u32 sb2 = slot_base(slot2, pol);
            st2((u32x2){SENT, SENT}, c.rs, px_vo, sb2);
            st2((u32x2){SENT, SENT}, c.rs, pm_vo, sb2);
            if (s + 1 < NSEG - 1) {
                if (!wait_ge(c, F_KX + h, s + 1)) return;
#pragma unroll
                for (int i = 0; i < 8; ++i) kx[i] = ld4(c.rs, OFF_KX + (u32)(h * 8 + i) * 1024 + lane * 16, (u32)(s + 1) * 262144u);
            }
        } else {
            __builtin_amdgcn_s_sleep(24);
        }
        slot = slot == 2 ? 0 : slot + 1;
    }
}

DEV void load_vo(const Params& P, const Ctx& c, int h, u32x8* wf) {
    if (c.wave < 4) load_w<10, 4096>(mk_rsrc(P.Wv, 640 * 4096 * 2), (u32)(h * 128 + 32 * c.wave) * 2, c.n, c.g, wf, wf + 10);
    else {
#pragma unroll
        for (int p = 0; p < 5; ++p) load_w<2, 640>(mk_rsrc(P.Wo, 640 * 4096 * 2), (u32)(h * 128 * 640 + 160 * (c.wave - 4) + 32 * p) * 2, c.n, c.g, wf + 4 * p, wf + 4 * p + 2);
    }
}
DEV void role_vom(const Params& P, const Ctx& c, int h, int rep) {
    const int w = c.wave, n = c.n, g = c.g, lane = c.lane;
    u32x8 wf[20];
    load_vo(P, c, h, wf);
    if (!wait_ge(c, F_POL, 0)) return;
    const u32 pol = __builtin_amdgcn_readfirstlane(__hip_atomic_load(flag(c, F_POL), RLX_AGENT));
    lchar* mimg = c.lds;
    lchar* vs = c.lds + 10240;
    const int idx = n < 8 ? 0x4444 : 0xEEEE;
    const u32 m_vo = (u32)((2 * rep + (w >> 2)) * 16384 + (w & 3) * 1280);
    const int ep = 16 * (w & 3) + n;
    lchar* v_wr = vs + (ep >> 5) * 1024 + (((ep >> 3) & 3) * 16 + 4 * (g & 1) + 2 * (g >> 1) + 8 * (ep & 1)) * 16 + ((ep & 7) >> 1) * 4;
    const int r0 = 10 * (w & 3) + (n >> 3);
    const u32 inner = (u32)(h * 256 + (8 * (g >> 1) + (n & 7)) * 16 + (g & 1) * 8);
    const u32 y_vo = (u32)(20 + r0) * 16384 + inner;
    const u32 yl_vo = OFF_YL + (u32)r0 * 8192 + inner;
    int slot = 0;
    for (int s = 0; s < NSEG; ++s) {
        const u32 sb = slot_base(slot, pol);
        if (!poll_m(c, m_vo, sb, mimg)) return;
        BAR();
        STAMP(32 + h, s, 0);
        if (w < 4) {
            u32 oa, ob;
            mem_proj(c, mimg, wf, wf + 10, idx, oa, ob);
            *(LAS u32*)(v_wr) = oa;
            *(LAS u32*)(v_wr + 16) = ob;
        }
        BAR();
        const int slot2 = slot == 0 ? 2 : slot - 1;
        if (w >= 4) {
            const u32x4 a0 = lds4(vs + lane * 16), a1 = lds4(vs + 1024 + lane * 16);
            u32x2 y[5];
#pragma unroll
            for (int p = 0; p < 5; ++p) {
                f32x4 d0 = {0.f, 0.f, 0.f, 0.f}, d1 = d0;
                d0 = smma(a0, wf[4 * p], d0, idx);     d1 = smma(a0, wf[4 * p + 2], d1, idx);
                d0 = smma(a1, wf[4 * p + 1], d0, idx); d1 = smma(a1, wf[4 * p + 3], d1, idx);
                y[p] = (u32x2){guard(pk(swap_add(d0[0], d1[0]), swap_add(d0[1], d1[1]))), guard(pk(swap_add(d0[2], d1[2]), swap_add(d0[3], d1[3])))};
                st2(y[p], c.rs, y_vo + p * 32768, sb);
            }
            FENCE();
            const u32 sb2 = slot_base(slot2, pol);
#pragma unroll
            for (int p = 0; p < 5; ++p) {
                st2(y[p], c.rs, yl_vo + p * 16384, (u32)s * 327680u);
                st2((u32x2){SENT, SENT}, c.rs, y_vo + p * 32768, sb2);
            }
            DRAIN();
            if (lane == 0) __hip_atomic_fetch_add(flag(c, F_SEG + s), 1u, RLX_AGENT);
        } else {
            STAMP(32 + h, s, 1);
            __builtin_amdgcn_s_sleep(24);
        }
        slot = slot == 2 ? 0 : slot + 1;
    }
}

DEV u32 probe(const Ctx& c, u32 off) {
    st1(0u, c.rs, off, 0);
    DRAIN();
    u64 a, b; u32 v;
    asm volatile("s_memtime %0\n\ts_waitcnt lgkmcnt(0)" : "=s"(a) :: "memory");
    v = ld1(c.rs, off, 0);
    asm volatile("s_waitcnt vmcnt(0)" : "+v"(v) :: "memory");
    asm volatile("s_memtime %0\n\ts_waitcnt lgkmcnt(0)" : "=s"(b) :: "memory");
    return (u32)(b - a);
}
DEV void role_red(const Params& P, const Ctx& c, int r, int rep) {
    const int w = c.wave, n = c.n, g = c.g, lane = c.lane;
    if (r == 0 && w == 0) {
        u32 pol = 0;
        for (int reg = 0; reg < 2; ++reg) {
            u32 best[2] = {~0u, ~0u};
            for (int i = 0; i < 6; ++i) {
                const u32 t = probe(c, (u32)reg * 2 * MB + 62 * 16384 + (i & 1) * 8192 + (i >> 1) * 1024);
                best[i & 1] = min(best[i & 1], t);
            }
            pol |= (best[1] < best[0] ? 1u : 0u) << reg;
        }
        if (lane == 0) __hip_atomic_store(flag(c, F_POL), pol, RLX_AGENT);
    }
    if (!wait_ge(c, F_POL, 0)) return;
    const u32 pol = __builtin_amdgcn_readfirstlane(__hip_atomic_load(flag(c, F_POL), RLX_AGENT));
    const int idx = n < 8 ? 0x4444 : 0xEEEE;
    const int oi = (lane >> 2) & 7, oq = 2 * (lane & 3) + (lane >> 5);
    const u32 mo = (u32)(r * 256 + lane * 4);
    const u32 m_vo = w < 4 ? (u32)((2 * w + (r >= 20)) * 16384) + mo - (r >= 20 ? 5120u : 0u) : OFF_MLOG + mo;
    if (w < 5) {
        const u32 v = *(const GAS u32*)(P.mem0 + oi * 640 + 16 * r + 2 * oq);
        st1(guard(v), c.rs, m_vo, w < 4 ? slot_base(0, pol) : 0u);
    }
    lchar* r_wr = c.lds + w * 528 + (4 * (g & 1) + 2 * (g >> 1)) * 64 + (g & 1) * 8 + (2 * (n & 7) + (n >> 3)) * 4;
    lchar* r_rd = c.lds + oi * 64 + (oi >> 2) * 8 + oq * 8;
    const u32 y_vo = (u32)((20 + r) * 16384 + w * 1024 + lane * 16);
    const u32 px_vo = (u32)((8 + 3 * rep + (w >> 2)) * 16384 + (w & 3) * 2048 + lane * 16);
    const u32 pm_vo = lane < 32 ? (u32)((8 + 3 * rep + 2) * 16384 + (4 * w + 2 * g) * 128 + n * 8) : 0x80000000u;
    const u32 u_vo = OFF_U + (u32)(r * 32 + 4 * w) * 1024 + lane * 16;
    int uprog = -1;
    int slot = 0;
    for (int s = 0; s < NSEG - 1; ++s) {
        if (uprog < s) {
            int sp = 0;
            for (;;) {
                FENCE();
                int v = (int)__hip_atomic_load(flag(c, F_U + 4 * w + (lane & 3)), RLX_AGENT);
                v = min(v, __shfl_xor(v, 1)); v = min(v, __shfl_xor(v, 2));
                uprog = __builtin_amdgcn_readfirstlane(v);
                if (uprog >= s) break;
                __builtin_amdgcn_s_sleep(2);
                if (give_up(c, sp)) return;
            }
        }
        const u32 ub = (u32)s * 1310720u;
        const u32x8 u0 = cat8(ld4(c.rs, u_vo, ub), ld4(c.rs, u_vo + 1024, ub));
        const u32x8 u1 = cat8(ld4(c.rs, u_vo + 2048, ub), ld4(c.rs, u_vo + 3072, ub));
        const u32 sb = slot_base(slot, pol);
        u32x4 y, p0, p1; u32x2 ma, mb;
        int sp = 0;
        for (;;) {
            FENCE();
            y = ld4(c.rs, y_vo, sb);
            if (__builtin_amdgcn_ballot_w64(umx4(y) == SENT) == 0) break;
            if (give_up(c, sp)) return;
        }
        for (;;) {
            FENCE();
            p0 = ld4(c.rs, px_vo, sb);
            p1 = ld4(c.rs, px_vo + 1024, sb);
            ma = ld2(c.rs, pm_vo, sb);
            mb = ld2(c.rs, pm_vo + 128, sb);
            const u32 mxv = umx3(umx3(umx4(p0), umx4(p1), ma.x), umx3(ma.y, mb.x, mb.y), 0u);
            if (__builtin_amdgcn_ballot_w64(mxv == SENT) == 0) break;
            if (give_up(c, sp)) return;
        }
        f32x4 d = {0.f, 0.f, 0.f, 0.f};
        d = smma(p0, u0, d, idx);
        d = smma(p1, u1, d, idx);
        d = smma((u32x4){ma.x, ma.y, mb.x, mb.y}, cat8(y, (u32x4){0u, 0u, 0u, 0u}), d, idx);
        const int par = (s & 1) * 4224;
        *(LAS float*)(r_wr + par) = swap_add(d[0], d[2]);
        *(LAS float*)(r_wr + par + 64) = swap_add(d[1], d[3]);
        BAR();
        STAMP(64 + r, s, 0);
        const int slotn = slot == 2 ? 0 : slot + 1;
        if (w < 5) {
            f32x2 a[8];
#pragma unroll
            for (int i = 0; i < 8; ++i) a[i] = *(LAS f32x2*)(r_rd + par + i * 528);
            const f32x2 t = ((a[0] + a[1]) + (a[2] + a[3])) + ((a[4] + a[5]) + (a[6] + a[7]));
            st1(guard(pk(t[0], t[1])), c.rs, m_vo, w < 4 ? slot_base(slotn, pol) : (u32)(s + 1) * 10240u);
            STAMP(64 + r, s, 1);
            FENCE();
            if (w < 4) st1(SENT, c.rs, m_vo, sb);
        }
        slot = slotn;
    }
}

DEV void role_kqx(const Params& P, const Ctx& c, int h) {
    const int w = c.wave, n = c.n, g = c.g, lane = c.lane;
    u32x8 w0[10], w1[10];
    u32x4 xr[5];
    x_load(c, P.x, 0, xr);
    load_w<10, 4096>(mk_rsrc(w < 4 ? P.Wq : P.Wk, 640 * 4096 * 2), (u32)(h * 128 + 32 * (w & 3)) * 2, n, g, w0, w1);
    lchar* ximg = c.lds;
    lchar* mimg = c.lds + 81920;
    lchar* ks_ = c.lds + 92160;
    lchar* ring = c.lds + 95232;
    const int idx = n < 8 ? 0x4444 : 0xEEEE;
    lchar* k_wr = ks_ + (w & 3) * 576 + (n >> 2) * 144 + (4 * (g & 1) + 2 * (g >> 1)) * 16 + (n & 3) * 4;
    lchar* k_rd = ks_ + g * 144 + (n & 7) * 16;
    const float cs = 0.08838834764831845f * LOG2E;
    x_store(c, ximg, xr);
    BAR();
    int rw = 0;
    for (int i = -KQ_LEAD; i < NSEG; ++i) {
        const int j = i + KQ_LEAD;
        if (j < NSEG) {
            x_load(c, P.x, j + 1, xr);
            u32x4 o[2];
            tok_proj(c, ximg + (j & 1) * 40960, w0, w1, o);
#pragma unroll
            for (int tt = 0; tt < 2; ++tt) {
                *(LAS u32x4*)(ring + rw * 16384 + (w >> 2) * 8192 + (tt * 4 + (w & 3)) * 1024 + lane * 16) = o[tt];
                if (w >= 4) st4(o[tt], c.rs, OFF_KX + (u32)(h * 8 + tt * 4 + (w & 3)) * 1024 + lane * 16, (u32)j * 262144u);
            }
            x_store(c, ximg + ((j + 1) & 1) * 40960, xr);
        }
        if (i >= 0) { if (!poll_m(c, OFF_MLOG + (u32)w * 1280, (u32)i * 10240u, mimg)) return; }
        DRAIN();
        BAR();
        if (c.tid == 0 && j < NSEG) __hip_atomic_store(flag(c, F_KX + h), (u32)j, RLX_AGENT);
        rw = rw == KQ_LEAD ? 0 : rw + 1;
        if (i < 0) continue;
        if (w >= 4) {
            u32 oa, ob;
            mem_proj(c, mimg, w0, w1, idx, oa, ob);
            *(LAS u32*)(k_wr) = oa;
            *(LAS u32*)(k_wr + 16) = ob;
        }
        u32x4 qf[4];
        f32x4 d0 = {0.f, 0.f, 0.f, 0.f}, d1 = d0, d2 = d0;
        if (w < 2) {
#pragma unroll
            for (int ks = 0; ks < 4; ++ks) {
                qf[ks] = lds4(ring + rw * 16384 + (w * 4 + ks) * 1024 + lane * 16);
                d0 = mma16(lds4(ring + rw * 16384 + 8192 + ks * 1024 + lane * 16), qf[ks], d0);
                d1 = mma16(lds4(ring + rw * 16384 + 12288 + ks * 1024 + lane * 16), qf[ks], d1);
            }
        }
        BAR();
        if (w < 2) {
#pragma unroll
            for (int ks = 0; ks < 4; ++ks) d2 = mma16(lds4(k_rd + ks * 576), qf[ks], d2);
            const float NEG = -1.0e9f / 0.08838834764831845f;
            const int a = 16 * w + n;
            float v[12];
#pragma unroll
            for (int r = 0; r < 4; ++r) {
                v[r] = (4 * g + r <= a) ? d0[r] : NEG;
                v[4 + r] = (16 + 4 * g + r <= a) ? d1[r] : NEG;
                v[8 + r] = g < 2 ? d2[r] : -INFINITY;
            }
            float mx = v[0];
#pragma unroll
            for (int r = 1; r < 12; ++r) mx = fmx(mx, v[r]);
            mx = max_x16(mx); mx = max_x32(mx);
            const float mc = -mx * cs;
            float sum = 0.f;
#pragma unroll
            for (int r = 0; r < 12; ++r) { v[r] = __builtin_amdgcn_exp2f(__builtin_fmaf(v[r], cs, mc)); sum += v[r]; }
            sum = add_x16(sum); sum = add_x32(sum);
            const float inv = 1.0f / sum;
            const u32 pb = (u32)i * 81920u;
            st4((u32x4){pk(v[0] * inv, v[1] * inv), pk(v[4] * inv, v[5] * inv), pk(v[2] * inv, v[3] * inv), pk(v[6] * inv, v[7] * inv)},
                c.rs, OFF_PT + (u32)(w * 32 + h) * 1024 + lane * 16, pb);
            st2((u32x2){pk(v[8] * inv, v[9] * inv), pk(v[10] * inv, v[11] * inv)}, c.rs,
                g < 2 ? OFF_PT + 65536u + (u32)(w * 8 + (h >> 2)) * 1024 + (u32)((h & 3) * 16 + n) * 16 + g * 8 : 0x80000000u, pb);
            DRAIN();
            if (lane == 0) __hip_atomic_fetch_add(flag(c, F_SEG + i), 1u, RLX_AGENT);
        }
    }
}

DEV void role_vox(const Params& P, const Ctx& c, int h) {
    const int w = c.wave, n = c.n, g = c.g, lane = c.lane;
    u32x8 wf[20];
    u32x4 xr[5];
    x_load(c, P.x, 0, xr);
    load_vo(P, c, h, wf);
    lchar* ximg = c.lds;
    lchar* vimg = c.lds + 81920;
    x_store(c, ximg, xr);
    BAR();
    const u32 u_vo = OFF_U + (u32)((10 * (w & 3) + (n >> 3)) * 32 + h) * 1024 + (u32)(g * 16 + (n & 7)) * 16;
    for (int i = 0; i <= NSEG; ++i) {
        x_load(c, P.x, i + 1, xr);
        if (w < 4) {
            if (i < NSEG) {
                u32x4 o[2];
                tok_proj(c, ximg + (i & 1) * 40960, wf, wf + 10, o);
#pragma unroll
                for (int tt = 0; tt < 2; ++tt) *(LAS u32x4*)(vimg + (i & 1) * 8192 + (tt * 4 + w) * 1024 + lane * 16) = o[tt];
            }
        } else if (i > 0) {
            u32x4 a[8];
#pragma unroll
            for (int k = 0; k < 8; ++k) a[k] = lds4(vimg + ((i - 1) & 1) * 8192 + k * 1024 + lane * 16);
#pragma unroll
            for (int p = 0; p < 5; ++p)
#pragma unroll
                for (int t = 0; t < 2; ++t) {
                    f32x4 d0 = {0.f, 0.f, 0.f, 0.f}, d1 = d0;
#pragma unroll
                    for (int ks = 0; ks < 4; ++ks) {
                        const u32x8& wv = wf[4 * p + 2 * t + (ks >> 1)];
                        const u32x4 b = (ks & 1) ? hi4(wv) : lo4(wv);
                        d0 = mma16(a[ks], b, d0);
                        d1 = mma16(a[4 + ks], b, d1);
                    }
                    st4((u32x4){pk(d0[0], d0[1]), pk(d1[0], d1[1]), pk(d0[2], d0[3]), pk(d1[2], d1[3])}, c.rs,
                        u_vo + p * 65536 + t * 128, (u32)(i - 1) * 1310720u);
                }
        }
        x_store(c, ximg + ((i + 1) & 1) * 40960, xr);
        if (w >= 4 && i > 0) {
            DRAIN();
            if (lane == 0) __hip_atomic_fetch_add(flag(c, F_SEG + i - 1), 1u, RLX_AGENT);
        }
        BAR();
        if (c.tid == 0 && i > 0) __hip_atomic_store(flag(c, F_U + h), (u32)(i - 1), RLX_AGENT);
    }
}

DEV bool wrk_barrier(const Ctx& c, int fi) {
    DRAIN();
    __syncthreads();
    LAS int* ok = (LAS int*)(c.lds + LDS_BYTES - 16);
    if (c.wave == 0) {
        if (c.lane == 0) {
            __builtin_amdgcn_fence(__ATOMIC_RELEASE, "agent");
            DRAIN();
            __hip_atomic_fetch_add(flag(c, fi), 1u, RLX_AGENT);
        }
        const bool good = wait_ge(c, fi, NWRK - 1);
        __builtin_amdgcn_fence(__ATOMIC_ACQUIRE, "agent");
        DRAIN();
        if (c.lane == 0) *ok = good ? 1 : 0;
    }
    __syncthreads();
    return *ok != 0;
}

DEV void wrk_qkv(const Params& P, const Ctx& c, int wk) {
    const int w = c.wave, n = c.n, g = c.g, lane = c.lane;
    GAS unsigned char* ws = (GAS unsigned char*)P.ws;
    if (wk >= 75) {
        const int gw_ = (wk - 75) * 8 + w;
        if (gw_ < 20) {
            for (int k = 0; k < 10; ++k) {
                u32x8 a, b;
                load_w<1, 640>(mk_rsrc(P.wo, 640 * 640 * 2), (u32)(k * 64 * 640 + 32 * gw_) * 2, n, g, &a, &b);
                GAS unsigned char* d = ws + OFF_WOF + (size_t)((2 * gw_ + (n >> 3)) * 20 + 2 * k) * 1024 + (g * 16 + (n & 7)) * 16;
                *(GAS u32x4*)(d) = lo4(a); *(GAS u32x4*)(d + 1024) = hi4(a);
                *(GAS u32x4*)(d + 128) = lo4(b); *(GAS u32x4*)(d + 1024 + 128) = hi4(b);
            }
        } else {
            u32 gv[5];
#pragma unroll
            for (int k = 0; k < 5; ++k) gv[k] = *(const GAS u32*)(P.gw + 2 * (lane + 64 * k));
            for (int t = gw_ - 20; t < 2048; t += 84) {
                float s = 0.f;
#pragma unroll
                for (int k = 0; k < 5; ++k) {
                    const u32 xv = *(const GAS u32*)(P.x + (size_t)t * 640 + 2 * (lane + 64 * k));
                    const u32 pr = pk(bf_lo(xv) * bf_lo(gv[k]), bf_hi(xv) * bf_hi(gv[k]));
                    s += bf_lo(pr) + bf_hi(pr);
                }
#pragma unroll
                for (int o = 32; o; o >>= 1) s += __shfl_xor(s, o);
                if (lane == 0) ((GAS float*)(ws + OFF_G))[t] = 1.0f / (1.0f + __expf(-s));
            }
        }
        return;
    }
    const int sl = wk % 15, rr = wk / 15;
    const int s0 = 13 * rr, s1 = min(s0 + 13, NSEG);
    const int kind = sl / 5;
    const int colw = 128 * (sl % 5) + 32 * (w & 3), tt = w >> 2;
    u32x8 w0[10], w1[10];
    u32x4 xr[5];
    x_load(c, P.x, s0, xr);
    load_w<10, 1920>(mk_rsrc(P.wqkv, 640 * 1920 * 2), (u32)(kind * 640 + colw) * 2, n, g, w0, w1);
    lchar* ximg = c.lds;
    x_store(c, ximg + (s0 & 1) * 40960, xr);
    BAR();
    const float qs = kind == 0 ? 0.22360679774997896f * LOG2E : 1.0f;
    u32 eo[2], es[2], eh[2];
#pragma unroll
    for (int hf = 0; hf < 2; ++hf) {
        const int t32 = 16 * tt + n;
        if (kind < 2) {
            const int col = colw + 8 * g + 4 * hf, hd = col / 20, d = col % 20;
            const u32 in0 = (u32)(((d >> 3) * 32 + t32) * 16 + (d & 4) * 2);
            if (kind == 0) { eo[hf] = d < 16 ? OFF_Q0 + (u32)hd * 65536 + in0 : OFF_Q1 + (u32)(hd * 2048 + t32) * 8; eh[hf] = d < 16 ? 16384 : 4096; }
            else { eo[hf] = OFF_KV + (u32)hd * 163840 + (d < 16 ? in0 : 16384 + (u32)t32 * 8); eh[hf] = 40960; }
            es[hf] = d < 16 ? 1024 : 256;
        } else {
            const int col = colw + 2 * n, hd = col / 20, d = col % 20;
            eo[hf] = OFF_KV + (u32)hd * 163840 + 20480 + (u32)(tt * 640 + ((g & 1) * 20 + d) * 16 + (g >> 1) * 8);
            es[hf] = 0; eh[hf] = 0;
        }
    }
    for (int s = s0; s < s1; ++s) {
        x_load(c, P.x, s + 1, xr);
        lchar* xi = ximg + (s & 1) * 40960 + tt * 20480 + lane * 16;
        f32x4 d0 = {0.f, 0.f, 0.f, 0.f}, d1 = d0;
        if (kind < 2) {
#pragma unroll
            for (int ks = 0; ks < 20; ++ks) {
                const u32x4 b = lds4(xi + ks * 1024);
                d0 = mma16((ks & 1) ? hi4(w0[ks >> 1]) : lo4(w0[ks >> 1]), b, d0);
                d1 = mma16((ks & 1) ? hi4(w1[ks >> 1]) : lo4(w1[ks >> 1]), b, d1);
            }
#pragma unroll
            for (int hf = 0; hf < 2; ++hf) {
                const u32x2 v = {pk(d0[2 * hf] * qs, d1[2 * hf] * qs), pk(d0[2 * hf + 1] * qs, d1[2 * hf + 1] * qs)};
                pst2(v, c.rs, eo[hf] + (u32)(s & 15) * es[hf] + (u32)(s >> 4) * eh[hf], 0);
            }
        } else {
#pragma unroll
            for (int ks = 0; ks < 20; ++ks) {
                const u32x4 a = lds4(xi + ks * 1024);
                d0 = mma16(a, (ks & 1) ? hi4(w0[ks >> 1]) : lo4(w0[ks >> 1]), d0);
                d1 = mma16(a, (ks & 1) ? hi4(w1[ks >> 1]) : lo4(w1[ks >> 1]), d1);
            }
            const u32 so = (u32)(s >> 4) * 40960u + (u32)(s & 15) * 1280u;
            pst2((u32x2){pk(d0[0], d0[1]), pk(d0[2], d0[3])}, c.rs, eo[0], so);
            pst2((u32x2){pk(d1[0], d1[1]), pk(d1[2], d1[3])}, c.rs, eo[0] + 16, so);
        }
        x_store(c, ximg + ((s + 1) & 1) * 40960, xr);
        BAR();
    }
}

constexpr int KVL = 16384 + 4096 + 32 * 672;
constexpr float RESCALE_THR = 6.0f;
DEV void wrk_attn(const Params& P, const Ctx& c, int wk) {
    const int w = c.wave, lane = c.lane, tid = c.tid;
    GAS unsigned char* ws = (GAS unsigned char*)P.ws;
    const int q32 = lane & 31, hh = lane >> 5;
    if (tid < 128) {
        const int b = tid >> 6, st = (tid >> 1) & 31, h2 = tid & 1;
        *(LAS u32x4*)(c.lds + b * KVL + 20480 + st * 672 + (h2 * 21 + 20) * 16) = (u32x4){0x3F803F80u, 0x3F803F80u, 0x3F803F80u, 0x3F803F80u};
    }
    int ldst[5];
#pragma unroll
    for (int k = 0; k < 5; ++k) {
        const int pc = tid + 512 * k;
        ldst[k] = pc < 1280 ? pc * 16 : 20480 + ((pc - 1280) + (pc - 1280) / 20) * 16;
    }
    const int k0_rd = lane * 16, k1_rd = 16384 + q32 * 8, v_rd = 20480 + (hh * 21 + min(q32, 20)) * 16;
    for (int u = wk; u < 256; u += NWRK) {
        const int h = u >> 3, qt = (u & 7) * 8 + w;
        const u32x4 q0 = *(const GAS u32x4*)(ws + OFF_Q0 + (size_t)(h * 64 + qt) * 1024 + lane * 16);
        u32x2 q1h = *(const GAS u32x2*)(ws + OFF_Q1 + (size_t)(h * 2048 + qt * 32 + q32) * 8);
        const u32x4 q1 = {hh ? 0u : q1h.x, hh ? 0u : q1h.y, 0u, 0u};
        const GAS u32x4* src = (const GAS u32x4*)(ws + OFF_KV + (size_t)h * 163840) + tid;
        u32x4 sr[5];
#pragma unroll
        for (int k = 0; k < 5; ++k) sr[k] = src[512 * k];
        BAR();
#pragma unroll
        for (int k = 0; k < 5; ++k) *(LAS u32x4*)(c.lds + ldst[k]) = sr[k];
        BAR();
        f32x16 o, cm;
#pragma unroll
        for (int i = 0; i < 16; ++i) { o[i] = 0.f; cm[i] = 0.f; }
        for (int ch = 0; ch < 4; ++ch) {
            if (ch < 3) {
#pragma unroll
                for (int k = 0; k < 5; ++k) sr[k] = src[(ch + 1) * 2560 + 512 * k];
            }
            lchar* L = c.lds + (ch & 1) * KVL;
#pragma unroll 2
            for (int t = 0; t < 16; ++t) {
                const u32x4 k0 = lds4(L + k0_rd + t * 1024);
                const u32x2 k1 = lds2(L + k1_rd + t * 256);
                const u32x4 v0 = lds4(L + v_rd + t * 1344), v1 = lds4(L + v_rd + t * 1344 + 672);
                f32x16 s = mma32(k0, q0, cm);
                s = mma32((u32x4){k1.x, k1.y, 0u, 0u}, q1, s);
                float pm = fmx(fmx(s[0], s[1]), s[2]);
#pragma unroll
                for (int i = 3; i < 15; i += 2) pm = fmx(fmx(pm, s[i]), s[i + 1]);
                pm = fmx(pm, s[15]);
                const bool first = (ch | t) == 0;
                if (first || __builtin_amdgcn_ballot_w64(!(pm <= RESCALE_THR)) != 0) {
                    float dl = max_x32(pm);
                    if (!first) dl = fmx(dl, 0.f);
                    const float f = __builtin_amdgcn_exp2f(-dl);
#pragma unroll
                    for (int i = 0; i < 16; ++i) { s[i] -= dl; cm[i] -= dl; }
                    if (!first) {
#pragma unroll
                        for (int i = 0; i < 16; ++i) o[i] *= f;
                    }
                }
                u32 p[8];
#pragma unroll
                for (int i = 0; i < 8; ++i) p[i] = pk(__builtin_amdgcn_exp2f(s[2 * i]), __builtin_amdgcn_exp2f(s[2 * i + 1]));
                o = mma32(v0, (u32x4){p[0], p[1], p[2], p[3]}, o);
                o = mma32(v1, (u32x4){p[4], p[5], p[6], p[7]}, o);
            }
            if (ch < 3) {
#pragma unroll
                for (int k = 0; k < 5; ++k) *(LAS u32x4*)(c.lds + ((ch + 1) & 1) * KVL + ldst[k]) = sr[k];
                BAR();
            }
        }
        auto lr = __builtin_amdgcn_permlane32_swap(__float_as_uint(o[8]), __float_as_uint(o[8]), false, false);
        const float inv = 1.0f / __uint_as_float(lr[1]);
#pragma unroll
        for (int b = 0; b < 3; ++b) {
            if (b == 2 && hh) break;
            const int col = 20 * h + 8 * b + 4 * hh;
            GAS unsigned char* dst = ws + OFF_OA + (size_t)((qt * 2 + (q32 >> 4)) * 20 + (col >> 5)) * 1024 + (((col >> 3) & 3) * 16 + (q32 & 15)) * 16 + (col & 4) * 2;
            *(GAS u32x2*)dst = (u32x2){pk(o[4 * b] * inv, o[4 * b + 1] * inv), pk(o[4 * b + 2] * inv, o[4 * b + 3] * inv)};
        }
    }
}

DEV void wrk_finish(const Params& P, const Ctx& c, int wk) {
    const int w = c.wave, n = c.n, g = c.g, lane = c.lane;
    GAS unsigned char* ws = (GAS unsigned char*)P.ws;
    LAS int* ok = (LAS int*)(c.lds + LDS_BYTES - 16);
    for (int u = wk; u < NSEG * 10; u += NWRK) {
        const int s = u / 10, cg = u % 10;
        f32x4 res[2][4];
        {
            f32x4 acc[2][4];
#pragma unroll
            for (int i = 0; i < 8; ++i) acc[i >> 2][i & 3] = (f32x4){0.f, 0.f, 0.f, 0.f};
#pragma unroll
            for (int k = 0; k < 3; ++k) {
                const int ks = w + 8 * k;
                if (ks < 20) {
                    u32x4 b[2], a[4];
#pragma unroll
                    for (int tt = 0; tt < 2; ++tt) b[tt] = *(const GAS u32x4*)(ws + OFF_OA + (size_t)((s * 2 + tt) * 20 + ks) * 1024 + lane * 16);
#pragma unroll
                    for (int ct = 0; ct < 4; ++ct) a[ct] = *(const GAS u32x4*)(ws + OFF_WOF + (size_t)((4 * cg + ct) * 20 + ks) * 1024 + lane * 16);
#pragma unroll
                    for (int i = 0; i < 8; ++i) acc[i >> 2][i & 3] = mma16(a[i & 3], b[i >> 2], acc[i >> 2][i & 3]);
                }
            }
#pragma unroll
            for (int i = 0; i < 8; ++i) res[i >> 2][i & 3] = acc[i >> 2][i & 3];
        }
        float gt[2];
#pragma unroll
        for (int tt = 0; tt < 2; ++tt) gt[tt] = ((const GAS float*)(ws + OFF_G))[32 * s + 16 * tt + n];
        if (w == 0) { const bool good = wait_ge(c, F_SEG + s, SEG_TARGET - 1); if (lane == 0) *ok = good ? 1 : 0; }
        BAR();
        if (*ok == 0) return;
        {
            f32x4 acc[2][4];
#pragma unroll
            for (int i = 0; i < 8; ++i) acc[i >> 2][i & 3] = (f32x4){0.f, 0.f, 0.f, 0.f};
            u32x4 b[5][2], a[5][4];
#pragma unroll
            for (int k = 0; k < 5; ++k) {
#pragma unroll
                for (int tt = 0; tt < 2; ++tt)
                    b[k][tt] = ld4(c.rs, OFF_PT + (k < 4 ? (u32)(tt * 32 + 4 * w + k) * 1024 : 65536u + (u32)(tt * 8 + w) * 1024) + lane * 16, (u32)s * 81920u);
#pragma unroll
                for (int ct = 0; ct < 4; ++ct)
                    a[k][ct] = k < 4 ? ld4(c.rs, OFF_U + (u32)((4 * cg + ct) * 32 + 4 * w + k) * 1024 + lane * 16, (u32)s * 1310720u)
                                     : ld4(c.rs, OFF_YL + (u32)((4 * cg + ct) * 8 + w) * 1024 + lane * 16, (u32)s * 327680u);
            }
#pragma unroll
            for (int k = 0; k < 5; ++k)
#pragma unroll
                for (int i = 0; i < 8; ++i) acc[i >> 2][i & 3] = mma16(a[k][i & 3], b[k][i >> 2], acc[i >> 2][i & 3]);
#pragma unroll
            for (int i = 0; i < 8; ++i) {
                const float gg = gt[i >> 2];
                res[i >> 2][i & 3] = res[i >> 2][i & 3] * gg + acc[i >> 2][i & 3] * (1.0f - gg);
                *(LAS f32x4*)(c.lds + (w * 8 + i) * 1024 + lane * 16) = res[i >> 2][i & 3];
            }
        }
        BAR();
        f32x4 t = *(LAS f32x4*)(c.lds + w * 1024 + lane * 16);
#pragma unroll
        for (int i = 1; i < 8; ++i) t += *(LAS f32x4*)(c.lds + (i * 8 + w) * 1024 + lane * 16);
        auto e0 = __builtin_amdgcn_permlane32_swap(__float_as_uint(t[0]), __float_as_uint(t[2]), false, false);
        auto e1 = __builtin_amdgcn_permlane32_swap(__float_as_uint(t[1]), __float_as_uint(t[3]), false, false);
        const u32x2 ov = {pk(__uint_as_float(e0[0]), __uint_as_float(e0[1])), pk(__uint_as_float(e1[0]), __uint_as_float(e1[1]))};
        const int tok = 32 * s + 16 * (w >> 2) + n, col = 64 * cg + 16 * (w & 3) + 8 * (g & 1) + 4 * (g >> 1);
        *(GAS u32x2*)(P.out + (size_t)tok * 640 + col) = ov;
    }
}

__global__ void __launch_bounds__(512, 2) mega_kernel(Params P) {
    extern __shared__ __attribute__((aligned(16))) unsigned char smem[];
    Ctx c;
    c.rs = __builtin_amdgcn_make_buffer_rsrc(P.ws, 0, (int)WS_BYTES, 0x00020000);
    c.fl = (gu32*)(P.ws + OFF_FLAG);
    c.t0 = rt();
    c.tid = threadIdx.x;
    c.wave = __builtin_amdgcn_readfirstlane(threadIdx.x >> 6);
    c.lane = threadIdx.x & 63;
    c.n = c.lane & 15;
    c.g = c.lane >> 4;
    c.lds = (lchar*)smem;
    const int b = blockIdx.x;
    const int idx = (b >> 3) * 4 + (b & 3);
    const int rep = (int)(__builtin_amdgcn_s_getreg(20 | (3 << 11)) & 3u);
    const int ro = (b & 4) == 0 ? idx : 128 + idx;
    if (ro < 32) role_qk(P, c, ro, rep);
    else if (ro < 64) role_vom(P, c, ro - 32, rep);
    else if (ro < 104) role_red(P, c, ro - 64, rep);
    else if (ro < 136) role_kqx(P, c, ro - 104);
    else if (ro < 168) role_vox(P, c, ro - 136);
    else {
        {
            const int wk = idx - 40;
            STAMP(104 + wk, 0, 0);
            wrk_qkv(P, c, wk);
            STAMP(104 + wk, 0, 1);
            if (!wrk_barrier(c, F_BAR1)) return;
            STAMP(104 + wk, 0, 2);
            wrk_attn(P, c, wk);
            STAMP(104 + wk, 0, 3);
            if (!wrk_barrier(c, F_BAR2)) return;
            STAMP(104 + wk, 1, 0);
            wrk_finish(P, c, wk);
            STAMP(104 + wk, 1, 1);
        }
    }
}

extern "C" void kernel_launch(void* const* d_in, const int* in_sizes, int n_in,
                              void* d_out, int out_size, void* d_ws, size_t ws_size,
                              hipStream_t stream) {
    (void)in_sizes; (void)n_in; (void)out_size; (void)ws_size;
    Params P;
    P.x = (const u16*)d_in[0];
    P.wqkv = (const u16*)d_in[1];
    P.wo = (const u16*)d_in[2];
    P.mem0 = (const u16*)d_in[4];
    P.Wq = (const u16*)d_in[5];
    P.Wk = (const u16*)d_in[6];
    P.Wv = (const u16*)d_in[7];
    P.Wo = (const u16*)d_in[8];
    P.gw = (const u16*)d_in[10];
    P.out = (u16*)d_out;
    P.ws = (unsigned char*)(((uintptr_t)d_ws + (2u * MB - 1)) & ~(uintptr_t)(2u * MB - 1));
    (void)hipFuncSetAttribute((const void*)mega_kernel, hipFuncAttributeMaxDynamicSharedMemorySize, LDS_BYTES);
    (void)hipMemsetAsync(P.ws, 0xFF, ARM_BYTES, stream);
    mega_kernel<<<256, 512, LDS_BYTES, stream>>>(P);
}
```

```cpp
#include <hip/hip_runtime.h>
#include <hip/hip_bf16.h>
#include <stdint.h>

typedef unsigned short u16;
typedef unsigned int u32;
typedef unsigned long long u64;
typedef __bf16 bf16x2 __attribute__((ext_vector_type(2)));
typedef __bf16 bf16x8 __attribute__((ext_vector_type(8)));
typedef __bf16 bf16x16 __attribute__((ext_vector_type(16)));
typedef float f32x2 __attribute__((ext_vector_type(2)));
typedef float f32x4 __attribute__((ext_vector_type(4)));
typedef float f32x16 __attribute__((ext_vector_type(16)));
typedef u32 u32x2 __attribute__((ext_vector_type(2)));
typedef u32 u32x4 __attribute__((ext_vector_type(4)));
typedef u32 u32x8 __attribute__((ext_vector_type(8)));
#define GAS __attribute__((address_space(1)))
#define LAS __attribute__((address_space(3)))
typedef GAS u32 gu32;
typedef LAS unsigned char lchar;
#define RLX_AGENT __ATOMIC_RELAXED, __HIP_MEMORY_SCOPE_AGENT
#define DEV __device__ __forceinline__

constexpr int NSEG = 64;
constexpr u32 SENT = 0xFFFFFFFFu;
constexpr u32 MB = 1u << 20;
constexpr u32 OFF_FLAG = 3 * MB;
constexpr u32 ARM_BYTES = OFF_FLAG + 131072;
constexpr u32 OFF_U = 4 * MB;
constexpr u32 OFF_YL = OFF_U + 80 * MB;
constexpr u32 OFF_KX = OFF_YL + 20 * MB;
constexpr u32 OFF_KM = OFF_KX + 16 * MB;
constexpr u32 OFF_PT = OFF_KM + 4 * MB;
constexpr u32 OFF_Q0 = OFF_PT + 5 * MB;
constexpr u32 OFF_Q1 = OFF_Q0 + 4 * MB;
constexpr u32 OFF_VT = OFF_Q1 + 1 * MB;
constexpr u32 OFF_OA = OFF_VT + 3 * MB;
constexpr u32 OFF_WOF = OFF_OA + 3 * MB;
constexpr u32 OFF_G = OFF_WOF + 1 * MB;
constexpr u32 OFF_DBG = OFF_G + 1 * MB;
constexpr u32 WS_BYTES = OFF_DBG + 2 * MB;
constexpr int F_POL = 0, F_BAR1 = 2, F_BAR2 = 3, F_U = 36, F_PT = 68, F_YL = 100, F_TW = 136;
constexpr int F_KX = 256, F_KM = 384;
constexpr int NWRK = 88;
constexpr int KQ_LEAD = 4;
constexpr int LDS_BYTES = 160 * 1024;
#define K_PE 44
#define KQ_NAP 6
#define QK_KD 3
#define T_D 146
#define T_RB 71
#define T_DA 8
#define T_YE 20
#define T_DS 60
#define T_FINE 1
#define T_UD 14
constexpr u32 SPIN_CAP = 1u << 21;
constexpr float LOG2E = 1.4426950408889634f;

struct Params {
    const u16 *x, *wqkv, *wo, *mem0, *Wq, *Wk, *Wv, *Wo, *gw;
    u16* out;
    unsigned char* ws;
};

typedef __amdgpu_buffer_rsrc_t rsrc_t;
DEV u64 rt() { return __builtin_amdgcn_s_memrealtime(); }
DEV u32 pk(float a, float b) { f32x2 v = {a, b}; return __builtin_bit_cast(u32, __builtin_convertvector(v, bf16x2)); }
DEV u32 guard(u32 v) { return v < 0xFFFFFFFEu ? v : 0xFFFFFFFEu; }
DEV float fmx(float a, float b) { return __builtin_elementwise_maximum(a, b); }
DEV u32 umx3(u32 a, u32 b, u32 c) { return max(max(a, b), c); }
DEV u32 umx4(u32x4 v) { return max(umx3(v.x, v.y, v.z), v.w); }
DEV float bf_lo(u32 v) { return __uint_as_float(v << 16); }
DEV float bf_hi(u32 v) { return __uint_as_float(v & 0xFFFF0000u); }
DEV u32x4 lo4(const u32x8& v) { return __builtin_shufflevector(v, v, 0, 1, 2, 3); }
DEV u32x4 hi4(const u32x8& v) { return __builtin_shufflevector(v, v, 4, 5, 6, 7); }
DEV u32x8 cat8(u32x4 a, u32x4 b) { return __builtin_shufflevector(a, b, 0, 1, 2, 3, 4, 5, 6, 7); }
DEV f32x4 mma16(u32x4 a, u32x4 b, f32x4 c) {
    return __builtin_amdgcn_mfma_f32_16x16x32_bf16(__builtin_bit_cast(bf16x8, a), __builtin_bit_cast(bf16x8, b), c, 0, 0, 0); }
DEV f32x16 mma32(u32x4 a, u32x4 b, f32x16 c) {
    return __builtin_amdgcn_mfma_f32_32x32x16_bf16(__builtin_bit_cast(bf16x8, a), __builtin_bit_cast(bf16x8, b), c, 0, 0, 0); }
DEV f32x4 smma(u32x4 a, const u32x8& b, f32x4 c, int idx) {
    return __builtin_amdgcn_smfmac_f32_16x16x64_bf16(__builtin_bit_cast(bf16x8, a), __builtin_bit_cast(bf16x16, b), c, idx, 0, 0); }
DEV float swap_add(float a, float b) {
    auto r = __builtin_amdgcn_permlane32_swap(__float_as_uint(a), __float_as_uint(b), false, false);
    return __uint_as_float(r[0]) + __uint_as_float(r[1]); }
DEV float ror8(float v) { return __uint_as_float(__builtin_amdgcn_update_dpp(0, __float_as_uint(v), 0x128, 0xf, 0xf, false)); }
DEV float max_x16(float v) { auto r = __builtin_amdgcn_permlane16_swap(__float_as_uint(v), __float_as_uint(v), false, false); return fmx(__uint_as_float(r[0]), __uint_as_float(r[1])); }
DEV float max_x32(float v) { auto r = __builtin_amdgcn_permlane32_swap(__float_as_uint(v), __float_as_uint(v), false, false); return fmx(__uint_as_float(r[0]), __uint_as_float(r[1])); }
DEV float add_x16(float v) { auto r = __builtin_amdgcn_permlane16_swap(__float_as_uint(v), __float_as_uint(v), false, false); return __uint_as_float(r[0]) + __uint_as_float(r[1]); }
DEV float add_x32(float v) { auto r = __builtin_amdgcn_permlane32_swap(__float_as_uint(v), __float_as_uint(v), false, false); return __uint_as_float(r[0]) + __uint_as_float(r[1]); }

DEV u32x4 ld4(rsrc_t rs, u32 vo, u32 so) { return __builtin_amdgcn_raw_buffer_load_b128(rs, vo, so, 16); }
DEV u32x2 ld2(rsrc_t rs, u32 vo, u32 so) { return __builtin_amdgcn_raw_buffer_load_b64(rs, vo, so, 16); }
DEV u32 ld1(rsrc_t rs, u32 vo, u32 so) { return __builtin_amdgcn_raw_buffer_load_b32(rs, vo, so, 16); }
DEV void st4(u32x4 v, rsrc_t rs, u32 vo, u32 so) { __builtin_amdgcn_raw_buffer_store_b128(v, rs, vo + so, 0, 16); }
DEV void st2(u32x2 v, rsrc_t rs, u32 vo, u32 so) { __builtin_amdgcn_raw_buffer_store_b64(v, rs, vo, so, 16); }
DEV void st1(u32 v, rsrc_t rs, u32 vo, u32 so) { __builtin_amdgcn_raw_buffer_store_b32(v, rs, vo, so, 16); }
DEV rsrc_t mk_rsrc(const void* p, u32 bytes) { return __builtin_amdgcn_make_buffer_rsrc((void*)p, 0, (int)bytes, 0x00020000); }
DEV u32x4 pld4(rsrc_t rs, u32 vo, u32 so) { return __builtin_amdgcn_raw_buffer_load_b128(rs, vo, so, 0); }
DEV u32x2 pld2(rsrc_t rs, u32 vo, u32 so) { return __builtin_amdgcn_raw_buffer_load_b64(rs, vo, so, 0); }
DEV u32 pld1(rsrc_t rs, u32 vo, u32 so) { return __builtin_amdgcn_raw_buffer_load_b32(rs, vo, so, 0); }
DEV void pst2(u32x2 v, rsrc_t rs, u32 vo, u32 so) { __builtin_amdgcn_raw_buffer_store_b64(v, rs, vo, so, 0); }
DEV u32x4 lds4(lchar* p) { return *(LAS u32x4*)p; }
DEV u32x2 lds2(lchar* p) { return *(LAS u32x2*)p; }
#define BAR() do { asm volatile("s_waitcnt lgkmcnt(0)" ::: "memory"); __builtin_amdgcn_s_barrier(); asm volatile("" ::: "memory"); } while (0)
#define DRAIN() asm volatile("s_waitcnt vmcnt(0)" ::: "memory")
#define FENCE() asm volatile("" ::: "memory")
#define SB() __builtin_amdgcn_sched_barrier(0)
#define PIN(...) asm volatile("" : __VA_ARGS__)

struct Ctx {
    rsrc_t rs;
    gu32* fl;
    int tid, wave, lane, n, g;
    lchar* lds;
};
DEV gu32* flag(const Ctx& c, int i) { return c.fl + i * 64; }
#define SPIN_TICK(cnt) asm volatile("s_sub_u32 %0, %0, 1\n\ts_cbranch_scc0 .Lsp%=\n\ts_endpgm\n.Lsp%=:" : "+s"(cnt) :: "scc", "memory")
template <int NAP = 2>
DEV int wait_ge(const Ctx& c, int fi, int want) {
    u32 sp = SPIN_CAP;
    int v = __builtin_amdgcn_readfirstlane(__hip_atomic_load(flag(c, fi), RLX_AGENT));
    while (v < want) {
        __builtin_amdgcn_s_sleep(NAP);
        SPIN_TICK(sp);
        v = __builtin_amdgcn_readfirstlane(__hip_atomic_load(flag(c, fi), RLX_AGENT));
    }
    return v;
}
DEV int prog4(const Ctx& c, int fi) {
    FENCE();
    int v = (int)__hip_atomic_load(flag(c, fi + (c.lane & 3)), RLX_AGENT);
    v = min(v, __shfl_xor(v, 1)); v = min(v, __shfl_xor(v, 2));
    return __builtin_amdgcn_readfirstlane(v);
}
template <int NAP>
DEV int wait4(const Ctx& c, int fi, int want) {
    u32 sp = SPIN_CAP;
    int v = prog4(c, fi);
    while (v < want) { __builtin_amdgcn_s_sleep(NAP); SPIN_TICK(sp); v = prog4(c, fi); }
    return v;
}
DEV u32 now32() { u64 t; asm volatile("s_memrealtime %0\n\ts_waitcnt lgkmcnt(0)" : "=s"(t) :: "memory"); return (u32)t; }
DEV void wait_until(u32 t) {
    int d = (int)(t - now32());
    if (d <= 0 || d > 1000) return;
    for (int i = 0; i < 400 && d > 3; ++i) d = (int)(t - now32());
    d = d < 0 ? 0 : d;
    if (d & 2) asm volatile("s_nop 10");
    if (d & 1) asm volatile("s_nop 4");
}
DEV u32 read_time(const Ctx& c, int s) {
    u32 v = SENT;
    for (int i = 0; i < 6 && v == SENT; ++i) { FENCE(); v = __builtin_amdgcn_readfirstlane(__hip_atomic_load(c.fl + (F_TW * 64 + s * 32), RLX_AGENT)); }
    return v == SENT ? now32() - 2000u : v;
}
DEV u32 time_word(const Ctx& c, int s) { return __hip_atomic_load(c.fl + (F_TW * 64 + s * 32), RLX_AGENT); }
DEV u32 mem_time(u32 tw) { tw = __builtin_amdgcn_readfirstlane(tw); return tw == SENT ? now32() : tw + (T_RB + T_DA); }
DEV u32 slot_base(int slot, u32 pol) { return (u32)slot * MB + (((pol >> (slot >> 1)) & 1u) << 13); }

#define TSTAMP(t, s, k) do { } while (0)
#define CSTAMP(s, k) do { } while (0)
#define DUMP(role_id) do { } while (0)
#define STAMP(role_id, s, k) TSTAMP(0, s, k)
#define PDECL do { } while (0)
#define PMARK(k) do { } while (0)
#define PDUMP(role_id) do { } while (0)
DEV u64* smem_dbg(const Ctx& c) { return (u64*)(LAS u64*)(c.lds + LDS_BYTES - 2048); }

template <int NK64, int LDW>
DEV void load_w(rsrc_t rw, u32 base, int n, int g, u32x8* w0, u32x8* w1) {
    const u32 vo = (u32)(8 * g * LDW + 2 * n) * 2;
#pragma unroll
    for (int k = 0; k < NK64; ++k) {
        u32 d[16];
#pragma unroll
        for (int j = 0; j < 16; ++j) d[j] = pld1(rw, vo, base + (u32)(64 * k + 32 * (j >> 3) + (j & 7)) * (LDW * 2));
#pragma unroll
        for (int j = 0; j < 8; ++j) {
            w0[k][j] = __builtin_amdgcn_perm(d[2 * j + 1], d[2 * j], 0x05040100);
            w1[k][j] = __builtin_amdgcn_perm(d[2 * j + 1], d[2 * j], 0x07060302);
        }
    }
}

DEV u32 poll_m_regs(const Ctx& c, u32 vo, u32 so, u32x4& v, u32& u) {
    u32 sp = SPIN_CAP;
    do {
        SPIN_TICK(sp);
        v = ld4(c.rs, vo + c.lane * 16, so);
        u = ld1(c.rs, vo + 1024 + c.lane * 4, so);
    } while (__builtin_amdgcn_ballot_w64(umx3(umx3(v.x, v.y, v.z), v.w, u) == SENT) != 0);
    return SPIN_CAP - 1 - sp;
}
DEV u32 poll_m(const Ctx& c, u32 vo, u32 so, lchar* d4, lchar* d1) {
    u32x4 v; u32 u;
    const u32 tries = poll_m_regs(c, vo, so, v, u);
    *(LAS u32x4*)d4 = v;
    *(LAS u32*)d1 = u;
    return tries;
}
DEV void mem_proj(lchar* p, const u32x8* w0, const u32x8* w1, int idx, f32x4 a0, f32x4 a1, u32& oa, u32& ob) {
    u32x4 a[10];
#pragma unroll
    for (int t = 0; t < 5; ++t) a[t] = lds4(p + t * 1024);
    SB();
#pragma unroll
    for (int t = 0; t < 10; ++t) {
        a0 = smma(a[t], w0[t], a0, idx);
        a1 = smma(a[t], w1[t], a1, idx);
        if (t < 5) a[t + 5] = lds4(p + (t + 5) * 1024);
        SB();
    }
    float s0 = swap_add(a0[0], a0[2]), s1 = swap_add(a0[1], a0[3]);
    PIN("+v"(s0), "+v"(s1));
    float s2 = swap_add(a1[0], a1[2]), s3 = swap_add(a1[1], a1[3]);
    PIN("+v"(s2), "+v"(s3));
    oa = pk(s0, s2);
    ob = pk(s1, s3);
}
#define ZERO4 ((f32x4){0.f, 0.f, 0.f, 0.f})
struct XDma { const u16* src; u32 la, vo; };
DEV XDma x_dma(const Ctx& c, const u16* x, int seg, lchar* dst, int rb) {
    XDma d;
    seg = seg < NSEG ? seg : NSEG - 1;
    d.src = x + (32 * seg + 8 * rb) * 640;
    d.la = __builtin_amdgcn_readfirstlane((u32)(uintptr_t)dst + (u32)rb * 10240);
    d.vo = (u32)((c.lane >> 3) * 1280 + ((c.lane & 7) ^ (c.lane >> 3)) * 16);
    return d;
}
template <int K>
DEV void x_piece(const XDma& d) {
    u32 keep;
    asm volatile("s_mov_b32 %0, m0\n\ts_add_u32 m0, %3, %4\n\ts_nop 0\n\tglobal_load_lds_dwordx4 %1, %2 offset:%5\n\ts_mov_b32 m0, %0"
                 : "=&s"(keep) : "v"(d.vo), "s"(d.src), "s"(d.la), "i"(K * 896), "i"(K * 128) : "memory", "scc");
}
DEV void x_piece_rt(const XDma& d, int k) {
    switch (k) { case 0: x_piece<0>(d); break; case 1: x_piece<1>(d); break; case 2: x_piece<2>(d); break; case 3: x_piece<3>(d); break;
                 case 4: x_piece<4>(d); break; case 5: x_piece<5>(d); break; case 6: x_piece<6>(d); break; case 7: x_piece<7>(d); break;
                 case 8: x_piece<8>(d); break; default: x_piece<9>(d); break; }
}
DEV void x_stage(const Ctx& c, const u16* x, int seg, lchar* dst, int rb) {
    const XDma d = x_dma(c, x, seg, dst, rb);
#pragma unroll
    for (int k = 0; k < 10; ++k) x_piece_rt(d, k);
}
DEV int x_lane_off(const Ctx& c) { return (c.n >> 3) * 10240 + (c.n & 7) * 128 + ((c.g ^ (c.n & 7)) * 16); }
DEV u32x4 x_frag(lchar* pe, lchar* po, int tt, int ks) { return lds4(((ks & 1) ? po : pe) + tt * 20480 + (ks >> 1) * 1024); }
template <bool DMA, bool FLAG, bool SWAP>
DEV void tok_acc(const Ctx& c, lchar* ximg, const u32x8* w0, const u32x8* w1, f32x4 (&acc)[2][2], const XDma& dm, gu32* fl, u32 fv) {
#pragma unroll
    for (int i = 0; i < 4; ++i) acc[i >> 1][i & 1] = (f32x4){0.f, 0.f, 0.f, 0.f};
    const int lo = x_lane_off(c);
    lchar* pe = ximg + lo;
    lchar* po = ximg + (lo ^ 64);
    u32x4 b[20][2];
#pragma unroll
    for (int ks = 0; ks < 3; ++ks) { b[ks][0] = x_frag(pe, po, 0, ks); b[ks][1] = x_frag(pe, po, 1, ks); }
    SB();
#pragma unroll
    for (int ks = 0; ks < 20; ++ks) {
        const u32x4 a0 = (ks & 1) ? hi4(w0[ks >> 1]) : lo4(w0[ks >> 1]);
        const u32x4 a1 = (ks & 1) ? hi4(w1[ks >> 1]) : lo4(w1[ks >> 1]);
#pragma unroll
        for (int tt = 0; tt < 2; ++tt) {
            acc[tt][0] = SWAP ? mma16(b[ks][tt], a0, acc[tt][0]) : mma16(a0, b[ks][tt], acc[tt][0]);
            acc[tt][1] = SWAP ? mma16(b[ks][tt], a1, acc[tt][1]) : mma16(a1, b[ks][tt], acc[tt][1]);
        }
        SB();
        if (ks + 3 < 20) { b[ks + 3][0] = x_frag(pe, po, 0, ks + 3); b[ks + 3][1] = x_frag(pe, po, 1, ks + 3); }
        if (DMA && ks < 10) x_piece_rt(dm, ks);
        if (FLAG && ks == 5) { DRAIN(); __hip_atomic_store(fl, fv, RLX_AGENT); }
        SB();
    }
}
template <bool DMA>
DEV void tok_proj(const Ctx& c, lchar* ximg, const u32x8* w0, const u32x8* w1, u32x4 (&o)[2], const XDma& dm, gu32* fl = nullptr, u32 fv = 0) {
    f32x4 acc[2][2];
    tok_acc<DMA, !DMA, false>(c, ximg, w0, w1, acc, dm, fl, fv);
#pragma unroll
    for (int tt = 0; tt < 2; ++tt)
#pragma unroll
        for (int r = 0; r < 4; ++r) o[tt][r] = pk(acc[tt][0][r], acc[tt][1][r]);
}

DEV u32 qk_p_vo(const Ctx& c, int h) {
    const int w = c.wave & 3, n = c.n, g = c.g;
    return (g & 1) == 0 ? (u32)((8 + 3 * w + (h >> 4)) * 16384 + ((h >> 1) & 7) * 1024 + ((2 * (h & 1) + (g >> 1)) * 16 + n) * 16)
                        : (c.lane < 24 ? (u32)((8 + 3 * w + 2) * 16384 + h * 128 + n * 16) : 0x80000000u);
}
DEV void role_q(const Params& P, const Ctx& c, int h, int rep) {
    const int w = c.wave, n = c.n, g = c.g, lane = c.lane;
    u32x8 w0[10], w1[10];
    load_w<10, 4096>(mk_rsrc(P.Wq, 640 * 4096 * 2), (u32)(h * 128 + 32 * w) * 2, n, g, w0, w1);
    const u32 pol = (u32)wait_ge(c, F_POL, 0);
    lchar* qs = c.lds + 10240;
    const int idx = n < 8 ? 0x4444 : 0xEEEE;
    const u32 m_vo = (u32)(2 * rep * 16384 + w * 1280);
    lchar* q_wr = qs + w * 576 + (n >> 2) * 144 + (4 * (g & 1) + 2 * (g >> 1)) * 16 + (n & 3) * 4;
    lchar* q_rd = qs + g * 144 + (n & 7) * 16;
    const int pr = (n & 1) | ((n & 2) << 1) | ((n & 4) >> 1);
    lchar* k_rd = qs + 2304 + g * 144 + pr * 16;
    const u32 p_vo = qk_p_vo(c, h);
    const float cs = 0.08838834764831845f * LOG2E;
    u32x4 kx[8];
    wait4<2>(c, F_KX + 4 * h, 0);
#pragma unroll
    for (int i = 0; i < 8; ++i) kx[i] = ld4(c.rs, OFF_KX + (u32)(h * 8 + i) * 1024 + lane * 16, 0);
    u32 t_m = now32();
    lchar* mrd = c.lds + lane * 16; lchar* md4 = mrd + w * 1280; lchar* md1 = c.lds + w * 1280 + 1024 + lane * 4;
    volatile LAS u32* qcnt = (volatile LAS u32*)(c.lds + 15360);
    if (c.tid < 8) qcnt[c.tid] = 0;
    int slot = 0;
    PDECL;
    for (int s = 0; s < NSEG; ++s) {
        const u32 sb = slot_base(slot, pol);
        f32x4 z0 = ZERO4, z1 = ZERO4;
        u32 p_cur = p_vo + sb;
        PIN("+v"(z0), "+v"(z1), "+v"(md4), "+v"(md1), "+v"(mrd), "+v"(p_cur));
        wait_until(t_m);
        TSTAMP(0, s, 2);
        const u32 tries = poll_m(c, m_vo, sb, md4, md1);
        PMARK(0);
        BAR();
        if (s == NSEG - 1) break;
        PMARK(1);
        STAMP(h, s, 0);
        u32 oa, ob;
        mem_proj(mrd, w0, w1, idx, z0, z1, oa, ob);
        *(LAS u32*)(q_wr) = oa;
        *(LAS u32*)(q_wr + 16) = ob;
        FENCE();
        qcnt[w] = (u32)(s + 1);
        PMARK(6);
        u32x4 q[4], km[4];
        u32 got, sp = SPIN_CAP;
        do {
            SPIN_TICK(sp);
            got = qcnt[lane & 3];
#pragma unroll
            for (int ks = 0; ks < 4; ++ks) q[ks] = *(volatile LAS u32x4*)(q_rd + ks * 576);
        } while (__builtin_amdgcn_ballot_w64(got != (u32)(s + 1)) != 0);
        PMARK(2);
        f32x4 d0 = ZERO4, d1 = ZERO4, d2 = ZERO4;
#pragma unroll
        for (int ks = 0; ks < 4; ++ks) {
            d0 = mma16(kx[ks], q[ks], d0);
            d1 = mma16(kx[4 + ks], q[ks], d1);
        }
        PIN("+v"(d0), "+v"(d1));
        BAR();
        PMARK(3);
#pragma unroll
        for (int ks = 0; ks < 4; ++ks) km[ks] = lds4(k_rd + ks * 576);
        SB();
        float v[4], e[4];
#pragma unroll
        for (int r = 0; r < 4; ++r) v[r] = n < 8 ? d0[r] : d1[r];
        float mx = fmx(fmx(fmx(v[0], v[1]), v[2]), v[3]);
        mx = fmx(mx, ror8(mx)); mx = max_x16(mx); mx = max_x32(mx);
        float mxc = mx * cs;
        PIN("+v"(mxc), "+v"(km[0]));
        SB();
#pragma unroll
        for (int ks = 0; ks < 4; ++ks) d2 = mma16(km[ks], q[ks], d2);
        SB();
#pragma unroll
        for (int r = 0; r < 4; ++r) v[r] = __builtin_amdgcn_exp2f(__builtin_fmaf(v[r], cs, -mxc));
        float sx = (v[0] + v[1]) + (v[2] + v[3]);
        sx += ror8(sx); sx = add_x16(sx); sx = add_x32(sx);
        PIN("+v"(sx), "+v"(d2));
        PMARK(4);
        const float lm = fmx(fmx(fmx(d2[0], d2[1]), d2[2]), d2[3]);
        const float mc = fmx(max_x16(lm), mx) * cs;
        const float fx = __builtin_amdgcn_exp2f(mxc - mc);
#pragma unroll
        for (int r = 0; r < 4; ++r) e[r] = __builtin_amdgcn_exp2f(__builtin_fmaf(d2[r], cs, -mc));
        const float inv = __builtin_amdgcn_rcpf(__builtin_fmaf(sx, fx, add_x16((e[0] + e[1]) + (e[2] + e[3]))));
        const float ix = inv * fx;
        u32 o0, o1, o2, o3;
        asm("v_cvt_pk_bf16_f32 %0, %4, |%5|\n\tv_cvt_pk_bf16_f32 %2, %8, |%9|\n\tv_cvt_pk_bf16_f32 %1, %6, |%7|\n\tv_cvt_pk_bf16_f32 %3, %10, |%11|\n\t"
            "v_permlane16_swap_b32 %0, %2\n\ts_nop 0\n\tv_permlane16_swap_b32 %1, %3"
            : "=&v"(o0), "=&v"(o1), "=&v"(o2), "=&v"(o3)
            : "v"(v[0] * ix), "v"(v[1] * ix), "v"(v[2] * ix), "v"(v[3] * ix), "v"(e[0] * inv), "v"(e[1] * inv), "v"(e[2] * inv), "v"(e[3] * inv));
        st4((u32x4){o0, o1, o2, o3}, c.rs, p_cur, 0);
        STAMP(h, s, 1);
        PMARK(5);
        do {
            SPIN_TICK(sp);
            got = qcnt[4 + (lane & 3)];
#pragma unroll
            for (int i = 0; i < 8; ++i) kx[i] = *(volatile LAS u32x4*)(mrd + 16384 + i * 1024);
        } while (__builtin_amdgcn_ballot_w64(got != (u32)(s + 1)) != 0);
        t_m = mem_time(qcnt[8]);
        slot = slot == 2 ? 0 : slot + 1;
    }
    PDUMP(h);
}
DEV void role_k(const Params& P, const Ctx& c, int h, int rep) {
    const int w = c.wave & 3, n = c.n, g = c.g, lane = c.lane;
    u32x8 w0[10], w1[10];
    load_w<10, 4096>(mk_rsrc(P.Wk, 640 * 4096 * 2), (u32)(h * 128 + 32 * w) * 2, n, g, w0, w1);
    const u32 pol = (u32)wait_ge(c, F_POL, 0);
    lchar* ks_ = c.lds + 10240 + 2304;
    const int idx = n < 8 ? 0x4444 : 0xEEEE;
    const u32 m_vo = (u32)((2 * rep + 1) * 16384 + w * 1280);
    lchar* k_wr = ks_ + w * 576 + (n >> 2) * 144 + (4 * (g & 1) + 2 * (g >> 1)) * 16 + (n & 3) * 4;
    const u32 p_vo = qk_p_vo(c, h);
    const u32 kx_vo = OFF_KX + (u32)(h * 8 + 2 * w) * 1024 + lane * 16;
    const u32 km_vo = lane < 32 ? OFF_KM + (u32)(h * 4 + w) * 512 + lane * 16 : 0x80000000u;
    lchar* km_rd = ks_ + w * 576 + ((lane >> 3) & 3) * 144 + (lane & 7) * 16;
    lchar* mrd = c.lds + lane * 16; lchar* md4 = mrd + (4 + w) * 1280; lchar* md1 = c.lds + (4 + w) * 1280 + 1024 + lane * 4;
    volatile LAS u32* qcnt = (volatile LAS u32*)(c.lds + 15360);
    gu32* f_km = flag(c, F_KM + 4 * h + w);
    int kprog = -1, slot = 0;
    u32 t_m = now32();
    PDECL;
    for (int s = 0; s < NSEG; ++s) {
        const u32 sb = slot_base(slot, pol);
        f32x4 z0 = ZERO4, z1 = ZERO4;
        u32 p_old = p_vo + slot_base(slot == 0 ? 2 : slot - 1, pol);
        PIN("+v"(z0), "+v"(z1), "+v"(md4), "+v"(md1), "+v"(mrd), "+v"(p_old));
        wait_until(t_m);
        poll_m(c, m_vo, sb, md4, md1);
        PMARK(0);
        BAR();
        PMARK(1);
        st4((u32x4){SENT, SENT, SENT, SENT}, c.rs, p_old, 0);
        __hip_atomic_store(f_km, (u32)(s - 1), RLX_AGENT);
        const bool kget = kprog > s;
        u32x4 k0, k1;
        if (kget) { k0 = ld4(c.rs, kx_vo, (u32)(s + 1) * 262144u); k1 = ld4(c.rs, kx_vo + 1024, (u32)(s + 1) * 262144u); }
        const u32 tw = time_word(c, s);
        __builtin_amdgcn_s_sleep(QK_KD);
        SB();
        u32 oa, ob;
        mem_proj(mrd, w0, w1, idx, z0, z1, oa, ob);
        *(LAS u32*)(k_wr) = oa;
        *(LAS u32*)(k_wr + 16) = ob;
        PMARK(2);
        if (s < NSEG - 1) BAR();
        st4(lds4(km_rd), c.rs, km_vo, (u32)s * 65536u);
        if (s == NSEG - 1) { DRAIN(); __hip_atomic_store(f_km, (u32)s, RLX_AGENT); break; }
        if (!kget && s + 1 < NSEG - 1) {
            wait4<2>(c, F_KX + 4 * h, s + 1);
            k0 = ld4(c.rs, kx_vo, (u32)(s + 1) * 262144u); k1 = ld4(c.rs, kx_vo + 1024, (u32)(s + 1) * 262144u);
        }
        *(LAS u32x4*)(mrd + 16384 + w * 2048) = k0;
        *(LAS u32x4*)(mrd + 16384 + w * 2048 + 1024) = k1;
        if (w == 0) qcnt[8] = tw;
        FENCE();
        qcnt[4 + w] = (u32)(s + 1);
        t_m = mem_time(tw);
        wait_until(t_m - K_PE);
        kprog = prog4(c, F_KX + 4 * h);
        slot = slot == 2 ? 0 : slot + 1;
    }
    PDUMP(h);
}

DEV void load_vo(const Params& P, const Ctx& c, int h, u32x8* wf) {
    if (c.wave < 4) load_w<10, 4096>(mk_rsrc(P.Wv, 640 * 4096 * 2), (u32)(h * 128 + 32 * c.wave) * 2, c.n, c.g, wf, wf + 10);
    else {
#pragma unroll
        for (int p = 0; p < 5; ++p) load_w<2, 640>(mk_rsrc(P.Wo, 640 * 4096 * 2), (u32)(h * 128 * 640 + 160 * (c.wave - 4) + 32 * p) * 2, c.n, c.g, wf + 4 * p, wf + 4 * p + 2);
    }
}
DEV void y_log(const Ctx& c, const u32x2 (&y)[5], u32 vo, u32 so) {
#pragma unroll
    for (int p = 0; p < 5; p += 2) {
        const u32x2 b = y[p < 4 ? p + 1 : p];
        auto r0 = __builtin_amdgcn_permlane16_swap(y[p].x, b.x, false, false);
        auto r1 = __builtin_amdgcn_permlane16_swap(y[p].y, b.y, false, false);
        st4((u32x4){r0[0], r1[0], r0[1], r1[1]}, c.rs, p == 4 && (c.g & 1) ? 0x80000000u : vo + p * 16384, so);
    }
}
DEV void role_vom(const Params& P, const Ctx& c, int h, int rep) {
    const int w = c.wave, n = c.n, g = c.g, lane = c.lane;
    u32x8 wf[20];
    if (w < 4) load_w<10, 4096>(mk_rsrc(P.Wv, 640 * 4096 * 2), (u32)(h * 128 + 32 * w) * 2, n, g, wf, wf + 10);
    else {
        const rsrc_t rw = mk_rsrc(P.Wo, 640 * 4096 * 2);
        const u32 vo = (u32)(4 * g * 640 + 2 * n) * 2;
#pragma unroll
        for (int q = 0; q < 20; ++q) {
            u32 d[8];
#pragma unroll
            for (int j = 0; j < 8; ++j)
                d[j] = pld1(rw, vo, (u32)((h * 128 + 32 * (q & 3) + 16 * (j >> 2) + (j & 3)) * 640 + 160 * (w - 4) + 32 * (q >> 2)) * 2);
#pragma unroll
            for (int u = 0; u < 4; ++u) {
                wf[q][2 * u] = __builtin_amdgcn_perm(d[2 * u + 1], d[2 * u], 0x05040100);
                wf[q][2 * u + 1] = __builtin_amdgcn_perm(d[2 * u + 1], d[2 * u], 0x07060302);
            }
        }
    }
    const u32 pol = (u32)wait_ge(c, F_POL, 0);
    lchar* mimg = c.lds;
    lchar* vs = c.lds + 10240;
    const int idx = n < 8 ? 0x4444 : 0xEEEE;
    const u32 m_vo = (u32)((2 * rep + (w >> 2)) * 16384 + (w & 3) * 1280);
    lchar* v_wr = vs + (w & 3) * 576 + (n >> 2) * 144 + (4 * (g & 1) + 2 * (g >> 1)) * 16 + (n & 3) * 4;
    lchar* v_rd = vs + g * 144 + (n & 7) * 16;
    lchar* md4 = mimg + w * 1280 + lane * 16; lchar* md1 = mimg + w * 1280 + 1024 + lane * 4; lchar* mrd = mimg + lane * 16;
    const int r0 = 10 * (w & 3) + (n >> 3);
    const u32 inner = (u32)(h * 256 + (8 * (g >> 1) + (n & 7)) * 16 + (g & 1) * 8);
    const u32 y_vo = (u32)(20 + r0) * 16384 + inner;
    const u32 yl_vo = OFF_YL + (u32)(r0 + 2 * (g & 1)) * 8192 + (inner & ~8u);
    const u32 ya_vo = (u32)(20 + 10 * (w & 3) + g) * 16384 + (u32)(h * 256 + n * 16);
    int slot = 0;
    u32 t_m = now32();
    PDECL;
    if (w < 4) {
        for (int s = 0; s < NSEG; ++s) {
            const u32 sb = slot_base(slot, pol);
            f32x4 z0 = ZERO4, z1 = ZERO4;
            PIN("+v"(z0), "+v"(z1), "+v"(md4), "+v"(md1), "+v"(mrd));
            wait_until(t_m);
            poll_m(c, m_vo, sb, md4, md1);
            PMARK(0);
            BAR();
            PMARK(1);
            STAMP(32 + h, s, 0);
            u32 oa, ob;
            mem_proj(mrd, wf, wf + 10, idx, z0, z1, oa, ob);
            *(LAS u32*)(v_wr) = oa;
            *(LAS u32*)(v_wr + 16) = ob;
            PMARK(2);
            BAR();
            t_m = mem_time(time_word(c, s));
            slot = slot == 2 ? 0 : slot + 1;
        }
    } else {
        u32x2 y[5];
        for (int s = 0; s < NSEG; ++s) {
            const u32 sb = slot_base(slot, pol);
            f32x4 d[5];
#pragma unroll
            for (int p = 0; p < 5; ++p) d[p] = ZERO4;
            PIN("+v"(d[0]), "+v"(d[1]), "+v"(d[2]), "+v"(d[3]), "+v"(d[4]), "+v"(md4), "+v"(md1));
            wait_until(t_m);
            poll_m(c, m_vo, sb, md4, md1);
            PMARK(0);
            BAR();
            PMARK(1);
            const u32 tw = time_word(c, s);
            if (w == 5 && s > 1) __hip_atomic_store(flag(c, F_YL + h), (u32)(s - 2), RLX_AGENT);
            if (s > 0) {
                y_log(c, y, yl_vo, (u32)(s - 1) * 327680u);
                const u32 sb2 = slot_base(slot == 0 ? 2 : slot - 1, pol);
#pragma unroll
                for (int p = 0; p < 3; ++p) st4((u32x4){SENT, SENT, SENT, SENT}, c.rs, p == 2 && g >= 2 ? 0x80000000u : ya_vo + p * 65536, sb2);
            }
            BAR();
            PMARK(3);
            u32x4 a[4];
#pragma unroll
            for (int ks = 0; ks < 4; ++ks) a[ks] = lds4(v_rd + ks * 576);
            SB();
#pragma unroll
            for (int p = 0; p <= 5; ++p) {
                if (p < 5) {
                    d[p] = smma(a[0], wf[4 * p], d[p], idx);
                    d[p] = smma(a[1], wf[4 * p + 1], d[p], idx);
                    d[p] = smma(a[2], wf[4 * p + 2], d[p], idx);
                    SB();
                }
                if (p > 0) {
                    y[p - 1] = (u32x2){guard(pk(d[p - 1][0], d[p - 1][1])), guard(pk(d[p - 1][2], d[p - 1][3]))};
                    st2(y[p - 1], c.rs, y_vo + (p - 1) * 32768, sb);
                    SB();
                }
                if (p < 5) { d[p] = smma(a[3], wf[4 * p + 3], d[p], idx); SB(); }
            }
            TSTAMP(256, s, 2);
            PMARK(4);
            t_m = mem_time(tw);
            slot = slot == 2 ? 0 : slot + 1;
        }
        y_log(c, y, yl_vo, (u32)(NSEG - 1) * 327680u);
    }
    DRAIN();
    BAR();
    if (c.tid == 0) __hip_atomic_store(flag(c, F_YL + h), (u32)(NSEG - 1), RLX_AGENT);
    PDUMP(32 + h);
}

DEV u32 probe(const Ctx& c, u32 off) {
    u64 a, b; u32 v;
    asm volatile("s_memtime %0\n\ts_waitcnt lgkmcnt(0)" : "=s"(a) :: "memory");
    v = ld1(c.rs, off, 0);
    asm volatile("s_waitcnt vmcnt(0)" : "+v"(v) :: "memory");
    asm volatile("s_memtime %0\n\ts_waitcnt lgkmcnt(0)" : "=s"(b) :: "memory");
    return (u32)(b - a);
}
DEV void role_red(const Params& P, const Ctx& c, int r, int rep) {
    const int w = c.wave, n = c.n, g = c.g, lane = c.lane;
    if (r == 0) {
        if (lane < 32) st1(0u, c.rs, (u32)(lane >> 4) * 2 * MB + 62 * 16384 + ((lane >> 3) & 1) * 8192 + (u32)(8 * w + (lane & 7)) * 128, 0);
        DRAIN();
        u32 best[4] = {~0u, ~0u, ~0u, ~0u};
        for (int i = 0; i < 4; ++i) {
#pragma unroll
            for (int k = 0; k < 4; ++k)
                best[k] = min(best[k], probe(c, (u32)(k >> 1) * 2 * MB + 62 * 16384 + (k & 1) * 8192 + (u32)(8 * w + i) * 128));
        }
        LAS u32* bs = (LAS u32*)(c.lds + 16384);
        if (lane == 0) *(LAS u32x4*)(bs + 4 * w) = (u32x4){best[0], best[1], best[2], best[3]};
        BAR();
        if (c.tid == 0) {
            for (int i = 1; i < 8; ++i)
                for (int k = 0; k < 4; ++k) best[k] = min(best[k], bs[4 * i + k]);
            __hip_atomic_store(flag(c, F_POL), (best[1] < best[0] ? 1u : 0u) | (best[3] < best[2] ? 2u : 0u), RLX_AGENT);
        }
    }
    const u32 pol = (u32)wait_ge(c, F_POL, 0);
    const int idx = n < 8 ? 0x4444 : 0xEEEE;
    const int oi = (lane >> 2) & 7, oq = 2 * (lane & 3) + (lane >> 5);
    const u32 mo = (u32)(r * 256 + lane * 4);
    const u32 m_vo = (u32)((2 * (w & 3) + (r >= 20)) * 16384) + mo - (r >= 20 ? 5120u : 0u);
    if (w < 4) {
        const u32 v = *(const GAS u32*)(P.mem0 + oi * 640 + 16 * r + 2 * oq);
        st1(guard(v), c.rs, m_vo, slot_base(0, pol));
    }
    lchar* r_wr = c.lds + ((4 * (g & 1) + 2 * (g >> 1) + 8 * (n & 1)) * 4 + ((n & 7) >> 1)) * 80 + w * 8 + (n >> 3) * 4;
    lchar* r_rd = c.lds + lane * 80;
    const u32 y_vo = (u32)((20 + r) * 16384 + w * 1024 + lane * 16);
    const u32 rot = (u32)(r >> 2) & 1u;
    const u32 px_vo = (u32)((8 + 3 * rep + (w >> 2)) * 16384 + (w & 3) * 2048 + lane * 16) + rot * 1024, px_vo1 = px_vo ^ 1024u;
    const u32 pm_vo = lane < 32 ? (u32)((8 + 3 * rep + 2) * 16384 + (4 * w + 2 * g) * 128 + (n & 7) * 16 + (n >> 3) * 8) : 0x80000000u;
    const u32 u_vo = OFF_U + (u32)(r * 32 + 4 * w) * 1024 + lane * 16 + rot * 2048, u_vo1 = u_vo ^ 2048u;
    int uprog = -1;
    PDECL;
    int slot = 0;
    u32 t_p = now32() - 2000u;
    for (int s = 0; s < NSEG - 1; ++s) {
        u32 sp = SPIN_CAP;
        const u32 sb = slot_base(slot, pol);
        const int slotn = slot == 2 ? 0 : slot + 1;
        __builtin_amdgcn_s_sleep(T_UD);
        while (uprog < s) { SPIN_TICK(sp); uprog = prog4(c, F_U + 4 * w); }
        const u32 ub = (u32)s * 1310720u;
        const u32x8 u0 = cat8(ld4(c.rs, u_vo, ub), ld4(c.rs, u_vo + 1024, ub));
        const u32x8 u1 = cat8(ld4(c.rs, u_vo1, ub), ld4(c.rs, u_vo1 + 1024, ub));
        if (s > 0) {
            if (w < 4) st1(SENT, c.rs, m_vo, slot_base(slot == 0 ? 2 : slot - 1, pol));
            t_p = read_time(c, s);
        }
        if (uprog == s) uprog = prog4(c, F_U + 4 * w);
        const u32 m_so = slot_base(slotn, pol);
        const int par = (s & 1) * 5120;
        u32x4 y, p0, p1; u32x2 ma, mb;
        f32x4 d = ZERO4;
        u32x4 zq = {0u, 0u, 0u, 0u};
        lchar* rw_ = r_wr + par; lchar* rr_ = r_rd + par;
        PIN("+v"(d), "+v"(zq), "+v"(rw_), "+v"(rr_));
        wait_until(t_p - T_YE);
        y = ld4(c.rs, y_vo, sb);
        wait_until(t_p);
        TSTAMP(0, s, 2);
        PMARK(1);
        for (;;) {
            FENCE();
            p0 = ld4(c.rs, px_vo, sb);
            p1 = ld4(c.rs, px_vo1, sb);
            ma = ld2(c.rs, pm_vo, sb);
            mb = ld2(c.rs, pm_vo + 128, sb);
            const u32 mxv = umx3(umx3(umx4(y), umx4(p0), umx4(p1)), umx3(ma.x, ma.y, mb.x), mb.y);
            if (__builtin_expect(__builtin_amdgcn_ballot_w64(mxv == SENT) == 0, 1)) break;
            SPIN_TICK(sp);
            y = ld4(c.rs, y_vo, sb);
        }
        PMARK(2);
        d = smma(p0, u0, d, idx);
        d = smma(p1, u1, d, idx);
        d = smma((u32x4){ma.x, ma.y, mb.x, mb.y}, cat8(y, zq), d, idx);
        *(LAS float*)(rw_) = swap_add(d[0], d[2]);
        *(LAS float*)(rw_ + 320) = swap_add(d[1], d[3]);
        PMARK(3);
        BAR();
        PMARK(4);
        STAMP(64 + r, s, 0);
        if (w < 4) {
            f32x4 a[4];
#pragma unroll
            for (int i = 0; i < 4; ++i) a[i] = *(LAS f32x4*)(rr_ + i * 16);
            const f32x4 t = (a[0] + a[1]) + (a[2] + a[3]);
            st1(guard(pk(t[0] + t[2], t[1] + t[3])), c.rs, m_vo, m_so);
            STAMP(64 + r, s, 1);
            PMARK(5);
        } else if (r == 0 && w == 5) {
            const u32 t = now32(), due = t_p + T_RB;
            const u32 nx = (int)(t - due) <= 0 ? due + T_D : t + (T_D + T_DS);
            if (lane == 0) __hip_atomic_store(c.fl + (F_TW * 64 + (s + 1) * 32), guard(nx), RLX_AGENT);
        }
        slot = slotn;
    }
    PDUMP(64 + r);
}

DEV void role_kqx(const Params& P, const Ctx& c, int h) {
    const int w = c.wave, n = c.n, g = c.g, lane = c.lane;
    u32x8 w0[10], w1[10];
    if (w < 4) x_stage(c, P.x, 0, c.lds, w);
    load_w<10, 4096>(mk_rsrc(w < 4 ? P.Wq : P.Wk, 640 * 4096 * 2), (u32)(h * 128 + 32 * (w & 3)) * 2, n, g, w0, w1);
    lchar* ximg = c.lds;
    lchar* ring = c.lds + 81920;
    const float cs = 0.08838834764831845f * LOG2E;
    const u32 km_vo = OFF_KM + (u32)h * 2048 + (u32)(g * 8 + (n & 7)) * 16;
    int mprog = -1;
    DRAIN();
    BAR();
    int rw = 0;
    for (int i = -KQ_LEAD; i < NSEG; ++i) {
        const int j = i + KQ_LEAD;
        u32x4 km[4];
        const bool early = w < 2 && i >= 0 && mprog >= i;
        if (early) {
#pragma unroll
            for (int ks = 0; ks < 4; ++ks) km[ks] = ld4(c.rs, km_vo + ks * 512, (u32)i * 65536u);
        }
        if (j < NSEG) {
            u32x4 o[2];
            const XDma dm = x_dma(c, P.x, j + 1, ximg + ((j + 1) & 1) * 40960, w & 3);
            if (w < 4) tok_proj<true>(c, ximg + (j & 1) * 40960, w0, w1, o, dm);
            else tok_proj<false>(c, ximg + (j & 1) * 40960, w0, w1, o, dm, flag(c, F_KX + 4 * h + (w & 3)), (u32)(j - 1));
#pragma unroll
            for (int tt = 0; tt < 2; ++tt) {
                *(LAS u32x4*)(ring + rw * 16384 + (w >> 2) * 8192 + (tt * 4 + (w & 3)) * 1024 + lane * 16) = o[tt];
                if (w >= 4) st4(o[tt], c.rs, OFF_KX + (u32)(h * 8 + tt * 4 + (w & 3)) * 1024 + lane * 16, (u32)j * 262144u);
            }
        }
        rw = rw == KQ_LEAD ? 0 : rw + 1;
        if (w < 2 && i >= 0) {
            f32x4 d0 = ZERO4, d1 = ZERO4, d2 = ZERO4;
            u32x4 qf[4];
#pragma unroll
            for (int ks = 0; ks < 4; ++ks) {
                qf[ks] = lds4(ring + rw * 16384 + (w * 4 + ks) * 1024 + lane * 16);
                d0 = mma16(lds4(ring + rw * 16384 + 8192 + ks * 1024 + lane * 16), qf[ks], d0);
                d1 = mma16(lds4(ring + rw * 16384 + 12288 + ks * 1024 + lane * 16), qf[ks], d1);
            }
            if (!early) {
                mprog = wait4<KQ_NAP>(c, F_KM + 4 * h, i);
#pragma unroll
                for (int ks = 0; ks < 4; ++ks) km[ks] = ld4(c.rs, km_vo + ks * 512, (u32)i * 65536u);
            }
#pragma unroll
            for (int ks = 0; ks < 4; ++ks) d2 = mma16(km[ks], qf[ks], d2);
            DRAIN();
            const float NEG = -1.0e9f / 0.08838834764831845f;
            const int a = 16 * w + n;
            float v[12];
#pragma unroll
            for (int r = 0; r < 4; ++r) {
                v[r] = (4 * g + r <= a) ? d0[r] : NEG;
                v[4 + r] = (16 + 4 * g + r <= a) ? d1[r] : NEG;
                v[8 + r] = g < 2 ? d2[r] : -INFINITY;
            }
            float mx = v[0];
#pragma unroll
            for (int r = 1; r < 12; ++r) mx = fmx(mx, v[r]);
            mx = max_x16(mx); mx = max_x32(mx);
            const float mc = -mx * cs;
            float sum = 0.f;
#pragma unroll
            for (int r = 0; r < 12; ++r) { v[r] = __builtin_amdgcn_exp2f(__builtin_fmaf(v[r], cs, mc)); sum += v[r]; }
            sum = add_x16(sum); sum = add_x32(sum);
            const float inv = __builtin_amdgcn_rcpf(sum);
            const u32 pb = (u32)i * 81920u;
            st4((u32x4){pk(v[0] * inv, v[1] * inv), pk(v[4] * inv, v[5] * inv), pk(v[2] * inv, v[3] * inv), pk(v[6] * inv, v[7] * inv)},
                c.rs, OFF_PT + (u32)(w * 32 + h) * 1024 + lane * 16, pb);
            st2((u32x2){pk(v[8] * inv, v[9] * inv), pk(v[10] * inv, v[11] * inv)}, c.rs,
                g < 2 ? OFF_PT + 65536u + (u32)(w * 8 + (h >> 2)) * 1024 + (u32)((h & 3) * 16 + n) * 16 + g * 8 : 0x80000000u, pb);
        } else if (w < 4) DRAIN();
        else if (j == 0) { DRAIN(); if (lane == 0) __hip_atomic_store(flag(c, F_KX + 4 * h + (w & 3)), 0u, RLX_AGENT); }
        BAR();
        if (c.tid == 192 && i > 0) __hip_atomic_store(flag(c, F_PT + h), (u32)(i - 1), RLX_AGENT);
    }
    DRAIN();
    BAR();
    if (c.tid == 0) __hip_atomic_store(flag(c, F_PT + h), (u32)(NSEG - 1), RLX_AGENT);
}

DEV void role_vox(const Params& P, const Ctx& c, int h) {
    const int w = c.wave, n = c.n, g = c.g, lane = c.lane;
    u32x8 wf[20];
    if (w < 4) x_stage(c, P.x, 0, c.lds, w);
    load_vo(P, c, h, wf);
    lchar* ximg = c.lds;
    lchar* vimg = c.lds + 81920;
    DRAIN();
    BAR();
    const u32 u_vo = OFF_U + (u32)((10 * (w & 3) + (n >> 3)) * 32 + h) * 1024 + (u32)(g * 16 + (n & 7)) * 16;
    for (int i = 0; i <= NSEG; ++i) {
        if (w < 4) {
            if (i < NSEG) {
                u32x4 o[2];
                tok_proj<true>(c, ximg + (i & 1) * 40960, wf, wf + 10, o, x_dma(c, P.x, i + 1, ximg + ((i + 1) & 1) * 40960, w));
#pragma unroll
                for (int tt = 0; tt < 2; ++tt) *(LAS u32x4*)(vimg + (i & 1) * 8192 + (tt * 4 + w) * 1024 + lane * 16) = o[tt];
            }
        } else if (i > 0) {
            u32x4 a[8];
#pragma unroll
            for (int k = 0; k < 8; ++k) a[k] = lds4(vimg + ((i - 1) & 1) * 8192 + k * 1024 + lane * 16);
            f32x4 d[11][2];
#pragma unroll
            for (int q = 0; q <= 10; ++q) {
                if (q < 10) {
                    d[q][0] = (f32x4){0.f, 0.f, 0.f, 0.f}; d[q][1] = d[q][0];
#pragma unroll
                    for (int ks = 0; ks < 2; ++ks) {
                        const u32x4 b = ks ? hi4(wf[2 * q]) : lo4(wf[2 * q]);
                        d[q][0] = mma16(a[ks], b, d[q][0]);
                        d[q][1] = mma16(a[4 + ks], b, d[q][1]);
                    }
                    SB();
                }
                if (q > 0) {
                    const f32x4 e0 = d[q - 1][0], e1 = d[q - 1][1];
                    st4((u32x4){pk(e0[0], e0[1]), pk(e1[0], e1[1]), pk(e0[2], e0[3]), pk(e1[2], e1[3])}, c.rs,
                        u_vo + ((q - 1) >> 1) * 65536 + ((q - 1) & 1) * 128, (u32)(i - 1) * 1310720u);
                }
                if (q < 10) {
#pragma unroll
                    for (int ks = 2; ks < 4; ++ks) {
                        const u32x4 b = (ks & 1) ? hi4(wf[2 * q + 1]) : lo4(wf[2 * q + 1]);
                        d[q][0] = mma16(a[ks], b, d[q][0]);
                        d[q][1] = mma16(a[4 + ks], b, d[q][1]);
                    }
                    SB();
                }
            }
        }
        const bool full = i <= 1 || i == NSEG;
        if (i > 0) { TSTAMP(0, i - 1, 1); TSTAMP(256, i - 1, 3); }
        if (w < 4 || full) DRAIN(); else asm volatile("s_waitcnt vmcnt(10)" ::: "memory");
        BAR();
        if (c.tid == 0 && i > 0) __hip_atomic_store(flag(c, F_U + h), (u32)(full ? i - 1 : i - 2), RLX_AGENT);
        if (i > 0) { STAMP(224 + h, i - 1, 0); }
    }
}

DEV void wrk_barrier(const Ctx& c, int fi, int arrivals) {
    DRAIN();
    BAR();
    if (c.wave == 0) {
        if (c.lane == 0) __hip_atomic_fetch_add(flag(c, fi), 1u, RLX_AGENT);
        wait_ge<16>(c, fi, arrivals - 1);
    }
    BAR();
}

DEV void side_jobs(const Params& P, const Ctx& c, int wid) {
    const int n = c.n, g = c.g;
    const rsrc_t rw = mk_rsrc(P.wo, 640 * 640 * 2), rx = mk_rsrc(P.x, 2048 * 640 * 2);
#pragma unroll 4
    for (int i = 0; i < 11; ++i) {
        const int t = wid + 44 * i, cg = t / 24, ks = t % 24, ga = 4 * ks + g, hd = ga / 3, u = ga % 3;
        u32 d[8];
#pragma unroll
        for (int j = 0; j < 8; ++j) {
            const int dd = u < 2 ? 4 * u + (j & 3) + 8 * (j >> 2) : 16 + j;
            d[j] = pld1(rw, dd < 20 && t < 480 ? (u32)((20 * hd + dd) * 640 + 32 * cg + 2 * n) * 2 : 0x80000000u, 0);
        }
        u32x4 a, b;
#pragma unroll
        for (int j = 0; j < 4; ++j) { a[j] = __builtin_amdgcn_perm(d[2 * j + 1], d[2 * j], 0x05040100); b[j] = __builtin_amdgcn_perm(d[2 * j + 1], d[2 * j], 0x07060302); }
        const u32 o = t < 480 ? OFF_WOF + (u32)((2 * cg + (n >> 3)) * 24 + ks) * 1024 + (u32)(g * 16 + (n & 7)) * 16 : 0x80000000u;
        st4(a, c.rs, o, 0);
        st4(b, c.rs, o + 128, 0);
    }
    u32x4 gv[5];
#pragma unroll
    for (int k = 0; k < 5; ++k) gv[k] = *(const GAS u32x4*)(P.gw + 8 * (n + 16 * k));
#pragma unroll 4
    for (int i = 0; i < 12; ++i) {
        const int t = (wid + 44 * i) * 4 + g;
        float s = 0.f;
#pragma unroll
        for (int k = 0; k < 5; ++k) {
            const u32x4 xv = pld4(rx, (u32)t * 1280 + (u32)(n + 16 * k) * 16, 0);
#pragma unroll
            for (int j = 0; j < 4; ++j) {
                const u32 pr = pk(bf_lo(xv[j]) * bf_lo(gv[k][j]), bf_hi(xv[j]) * bf_hi(gv[k][j]));
                s += bf_lo(pr) + bf_hi(pr);
            }
        }
#pragma unroll
        for (int o = 8; o; o >>= 1) s += __shfl_xor(s, o);
        st1(__float_as_uint(1.0f / (1.0f + __expf(-s))), c.rs, n == 0 && t < 2048 ? OFF_G + (u32)t * 4 : 0x80000000u, 0);
    }
}
DEV void wrk_qkv(const Params& P, const Ctx& c, int wk) {
    const int w = c.wave, n = c.n, g = c.g;
    const int G60 = 8 * (wk & 7) + w, rr = wk >> 3;
    const int s0 = 6 * rr, s1 = min(s0 + 6, NSEG);
    const int kind = G60 / 20, G = G60 % 20;
    u32x8 w0[10], w1[10];
    if (w < 4) x_stage(c, P.x, s0, c.lds + (s0 & 1) * 40960, w);
    int cp = 16 * G + n;
    u32 eo, es;
    if (kind < 2) {
        const int gi = 4 * G + (n >> 2), k = gi % 5;
        cp = 20 * (gi / 5) + (k < 4 ? 10 * (k >> 1) + 4 * (k & 1) + (n & 3) : 10 * ((n >> 1) & 1) + 8 + (n & 1));
        const int go = 4 * G + g, pr = go / 5, ko = go % 5;
        eo = ko < 4 ? OFF_Q0 + (u32)(kind * 32 + 2 * pr + (ko >> 1)) * 65536 + (u32)((ko & 1) * 32 + n) * 16
                    : OFF_Q1 + (u32)(kind * 16 + pr) * 32768 + (u32)n * 16;
        es = ko < 4 ? 1024 : 512;
    } else {
        eo = OFF_VT + (u32)((g & 1) * 2 + (g >> 1)) * 5120 + (u32)cp * 16;
        es = 40960;
    }
    if (kind < 3) load_w<10, 1920>(mk_rsrc(P.wqkv, 640 * 1920 * 2), (u32)kind * 1280, cp, g, w0, w1);
    else side_jobs(P, c, rr * 4 + (w - 4));
    lchar* ximg = c.lds;
    DRAIN();
    BAR();
    const float qs = kind == 0 ? 0.22360679774997896f * LOG2E : 1.0f;
    for (int s = s0; s < s1; ++s) {
        const XDma dm = x_dma(c, P.x, s + 1, ximg + ((s + 1) & 1) * 40960, w & 3);
        f32x4 acc[2][2];
        if (kind < 2) {
            if (w < 4) tok_acc<true, false, false>(c, ximg + (s & 1) * 40960, w0, w1, acc, dm, nullptr, 0);
            else tok_acc<false, false, false>(c, ximg + (s & 1) * 40960, w0, w1, acc, dm, nullptr, 0);
#pragma unroll
            for (int tt = 0; tt < 2; ++tt)
                st4((u32x4){pk(acc[tt][0][0] * qs, acc[tt][1][0] * qs), pk(acc[tt][0][1] * qs, acc[tt][1][1] * qs),
                            pk(acc[tt][0][2] * qs, acc[tt][1][2] * qs), pk(acc[tt][0][3] * qs, acc[tt][1][3] * qs)}, c.rs, eo + (u32)s * es + tt * 256, 0);
        } else if (kind == 2) {
            if (w < 4) tok_acc<true, false, true>(c, ximg + (s & 1) * 40960, w0, w1, acc, dm, nullptr, 0);
            else tok_acc<false, false, true>(c, ximg + (s & 1) * 40960, w0, w1, acc, dm, nullptr, 0);
#pragma unroll
            for (int tt = 0; tt < 2; ++tt) {
                auto r0 = __builtin_amdgcn_permlane32_swap(pk(acc[tt][0][0], acc[tt][0][1]), pk(acc[tt][1][0], acc[tt][1][1]), false, false);
                auto r1 = __builtin_amdgcn_permlane32_swap(pk(acc[tt][0][2], acc[tt][0][3]), pk(acc[tt][1][2], acc[tt][1][3]), false, false);
                st4((u32x4){r0[0], r1[0], r0[1], r1[1]}, c.rs, eo + (u32)s * es + tt * 20480, 0);
            }
        }
        asm volatile("s_waitcnt vmcnt(2)" ::: "memory");
        BAR();
    }
}

constexpr int KVL = 16384 + 4096 + 32 * 672;
constexpr float RESCALE_THR = 6.0f;
DEV float row_max16(const f32x16& s) {
    const float a = fmx(fmx(fmx(fmx(s[0], s[1]), s[2]), fmx(fmx(s[3], s[4]), s[5])), fmx(fmx(s[6], s[7]), s[8]));
    const float b = fmx(fmx(fmx(s[9], s[10]), s[11]), fmx(fmx(s[12], s[13]), s[14]));
    return fmx(fmx(a, b), s[15]);
}
DEV void attn_step(lchar* vp, lchar* k0p, lchar* k1p, const u32x4& q0, const u32x4& q1, u32x4& k0, u32x2& k1,
                   const f32x16& cur, f32x16& nxt, f32x16& cm, f32x16& o, bool last = false) {
    const u32x4 v0 = lds4(vp), v1 = lds4(vp + 672);
    float e[16];
    u32 p[8];
    nxt = mma32(k0, q0, cm);
    SB();
    k0 = lds4(k0p);
#pragma unroll
    for (int i = 0; i < 4; ++i) e[i] = __builtin_amdgcn_exp2f(cur[i]);
    SB();
    nxt = mma32((u32x4){k1.x, k1.y, 0u, 0u}, q1, nxt);
    SB();
    k1 = lds2(k1p);
#pragma unroll
    for (int i = 4; i < 8; ++i) e[i] = __builtin_amdgcn_exp2f(cur[i]);
#pragma unroll
    for (int i = 0; i < 4; ++i) p[i] = pk(e[2 * i], e[2 * i + 1]);
#pragma unroll
    for (int i = 8; i < 10; ++i) e[i] = __builtin_amdgcn_exp2f(cur[i]);
    SB();
    o = mma32(v0, (u32x4){p[0], p[1], p[2], p[3]}, o);
    SB();
#pragma unroll
    for (int i = 10; i < 16; ++i) e[i] = __builtin_amdgcn_exp2f(cur[i]);
#pragma unroll
    for (int i = 4; i < 8; ++i) p[i] = pk(e[2 * i], e[2 * i + 1]);
    const float m0 = fmx(fmx(nxt[0], nxt[1]), nxt[2]), m1 = fmx(fmx(nxt[3], nxt[4]), nxt[5]);
    SB();
    o = mma32(v1, (u32x4){p[4], p[5], p[6], p[7]}, o);
    SB();
    const float pm = fmx(fmx(fmx(fmx(m0, m1), fmx(fmx(nxt[6], nxt[7]), nxt[8])), fmx(fmx(fmx(nxt[9], nxt[10]), nxt[11]), fmx(fmx(nxt[12], nxt[13]), nxt[14]))), nxt[15]);
    if (__builtin_expect(__builtin_amdgcn_ballot_w64(!(pm <= RESCALE_THR)) != 0 && !last, 0)) {
        const float dl = fmx(max_x32(pm), 0.f);
        const float f = __builtin_amdgcn_exp2f(-dl);
#pragma unroll
        for (int i = 0; i < 16; ++i) { nxt[i] -= dl; cm[i] -= dl; o[i] *= f; }
    }
}
DEV void wrk_attn(const Params& P, const Ctx& c, int wk) {
    const int w = c.wave, lane = c.lane, tid = c.tid;
    const int q32 = lane & 31, hh = lane >> 5;
    if (tid < 128) {
        const int b = tid >> 6, st = (tid >> 1) & 31, h2 = tid & 1;
        *(LAS u32x4*)(c.lds + b * KVL + 20480 + st * 672 + (h2 * 21 + 20) * 16) = (u32x4){0x3F803F80u, 0x3F803F80u, 0x3F803F80u, 0x3F803F80u};
    }
    int vdst[3]; u32 vsrc[3];
#pragma unroll
    for (int k = 0; k < 3; ++k) {
        const int v = tid + 512 * k, sh = v / 20, d = v % 20;
        vdst[k] = v < 1280 ? 20480 + (v + sh) * 16 : 2 * KVL + 16 * (tid & 255);
        vsrc[k] = v < 1280 ? OFF_VT + (u32)(sh * 2 + (d & 1)) * 5120 + (u32)(d >> 1) * 16 : 0x80000000u;
    }
    const int k0_rd = lane * 16, k1_rd = 16384 + q32 * 8, v_rd = 20480 + (hh * 21 + min(q32, 20)) * 16;
    for (int u = wk; u < 256; u += NWRK) {
        const int h = u >> 3, qt = (u & 7) * 8 + w;
        const u32x4 q0 = ld4(c.rs, OFF_Q0 + lane * 16, (u32)(h * 64 + qt) * 1024);
        const u32x2 q1h = ld2(c.rs, OFF_Q1 + q32 * 16, (u32)((h >> 1) * 2048 + qt * 32) * 16 + (h & 1) * 8);
        const u32x4 q1 = {hh ? 0u : q1h.x, hh ? 0u : q1h.y, 0u, 0u};
        u32x4 sr[5]; u32x2 sk;
        auto fetch = [&](int ch) {
            sr[0] = ld4(c.rs, OFF_Q0 + tid * 16, (u32)((32 + h) * 4 + ch) * 16384);
            sr[1] = ld4(c.rs, OFF_Q0 + 8192 + tid * 16, (u32)((32 + h) * 4 + ch) * 16384);
            sk = ld2(c.rs, OFF_Q1 + tid * 16, (u32)((16 + (h >> 1)) * 4 + ch) * 8192 + (h & 1) * 8);
#pragma unroll
            for (int k = 0; k < 3; ++k) sr[2 + k] = ld4(c.rs, vsrc[k], (u32)ch * 655360u + (u32)h * 160);
        };
        auto put = [&](int buf) {
            lchar* B = c.lds + buf * KVL;
            *(LAS u32x4*)(B + tid * 16) = sr[0];
            *(LAS u32x4*)(B + 8192 + tid * 16) = sr[1];
            *(LAS u32x2*)(B + 16384 + tid * 8) = sk;
#pragma unroll
            for (int k = 0; k < 3; ++k) *(LAS u32x4*)(B + vdst[k]) = sr[2 + k];
        };
        fetch(0);
        BAR();
        put(0);
        BAR();
        f32x16 o, cm, sa, sb;
#pragma unroll
        for (int i = 0; i < 16; ++i) { o[i] = 0.f; cm[i] = 0.f; }
        u32x4 k0 = lds4(c.lds + k0_rd);
        u32x2 k1 = lds2(c.lds + k1_rd);
        sa = mma32(k0, q0, cm);
        sa = mma32((u32x4){k1.x, k1.y, 0u, 0u}, q1, sa);
        k0 = lds4(c.lds + k0_rd + 1024);
        k1 = lds2(c.lds + k1_rd + 256);
        {
            const float dl = max_x32(row_max16(sa));
#pragma unroll
            for (int i = 0; i < 16; ++i) { sa[i] -= dl; cm[i] -= dl; }
        }
        for (int ch = 0; ch < 4; ++ch) {
            if (ch < 3) fetch(ch + 1);
            lchar* L = c.lds + (ch & 1) * KVL;
            lchar* Ln = c.lds + ((ch + 1) & 1) * KVL;
            for (int t = 0; t < 14; t += 2) {
                attn_step(L + v_rd + t * 1344, L + k0_rd + (t + 2) * 1024, L + k1_rd + (t + 2) * 256, q0, q1, k0, k1, sa, sb, cm, o);
                attn_step(L + v_rd + (t + 1) * 1344, L + k0_rd + (t + 3) * 1024, L + k1_rd + (t + 3) * 256, q0, q1, k0, k1, sb, sa, cm, o);
            }
            if (ch < 3) {
                put((ch + 1) & 1);
                BAR();
            }
            attn_step(L + v_rd + 14 * 1344, Ln + k0_rd, Ln + k1_rd, q0, q1, k0, k1, sa, sb, cm, o);
            attn_step(L + v_rd + 15 * 1344, Ln + k0_rd + 1024, Ln + k1_rd + 256, q0, q1, k0, k1, sb, sa, cm, o, ch == 3);
            if (ch < 3) BAR();
        }
        auto lr = __builtin_amdgcn_permlane32_swap(__float_as_uint(o[8]), __float_as_uint(o[8]), false, false);
        const float inv = 1.0f / __uint_as_float(lr[1]);
        const u32 ob = OFF_OA + (u32)(qt * 2 + (q32 >> 4)) * 24576 + (u32)(q32 & 15) * 16;
        st4((u32x4){pk(o[0] * inv, o[1] * inv), pk(o[2] * inv, o[3] * inv), pk(o[4] * inv, o[5] * inv), pk(o[6] * inv, o[7] * inv)}, c.rs, ob + (u32)(3 * h + hh) * 256, 0);
        st4((u32x4){pk(o[8] * inv, o[9] * inv), pk(o[10] * inv, o[11] * inv), 0u, 0u}, c.rs, hh ? 0x80000000u : ob + (u32)(3 * h + 2) * 256, 0);
    }
}

DEV void wrk_finish(const Params& P, const Ctx& c, int wk) {
    const int w = c.wave, n = c.n, g = c.g, lane = c.lane;
    int prog = -1;
    for (int u = wk; u < NSEG * 10; u += NWRK) {
        const int s = u / 10, cg = u % 10;
        f32x4 res[2][4];
        {
            f32x4 acc[2][4];
#pragma unroll
            for (int i = 0; i < 8; ++i) acc[i >> 2][i & 3] = (f32x4){0.f, 0.f, 0.f, 0.f};
#pragma unroll
            for (int k = 0; k < 3; ++k) {
                u32x4 b[2], a[4];
#pragma unroll
                for (int tt = 0; tt < 2; ++tt) b[tt] = ld4(c.rs, OFF_OA + lane * 16, (u32)((s * 2 + tt) * 24 + 3 * w + k) * 1024);
#pragma unroll
                for (int ct = 0; ct < 4; ++ct) a[ct] = ld4(c.rs, OFF_WOF + lane * 16, (u32)((4 * cg + ct) * 24 + 3 * w + k) * 1024);
#pragma unroll
                for (int i = 0; i < 8; ++i) acc[i >> 2][i & 3] = mma16(a[i & 3], b[i >> 2], acc[i >> 2][i & 3]);
            }
#pragma unroll
            for (int i = 0; i < 8; ++i) res[i >> 2][i & 3] = acc[i >> 2][i & 3];
        }
        float gt[2];
#pragma unroll
        for (int tt = 0; tt < 2; ++tt) gt[tt] = __uint_as_float(ld1(c.rs, OFF_G + (u32)(16 * tt + n) * 4, (u32)s * 128));
        if (prog < s) {
            u32 sp = SPIN_CAP;
            for (;;) {
                FENCE();
                const int v = (int)__hip_atomic_load(flag(c, F_U + 32 * min(lane >> 2, 2) + 4 * w + (lane & 3)), RLX_AGENT);
                if (__builtin_amdgcn_ballot_w64(v < s) == 0) { prog = s; if (__builtin_amdgcn_ballot_w64(v < NSEG - 1) == 0) prog = NSEG; break; }
                __builtin_amdgcn_s_sleep(8);
                SPIN_TICK(sp);
            }
        }
        {
            f32x4 acc[2][4];
#pragma unroll
            for (int i = 0; i < 8; ++i) acc[i >> 2][i & 3] = (f32x4){0.f, 0.f, 0.f, 0.f};
            u32x4 b[5][2], a[5][4];
#pragma unroll
            for (int k = 0; k < 5; ++k) {
#pragma unroll
                for (int tt = 0; tt < 2; ++tt)
                    b[k][tt] = ld4(c.rs, OFF_PT + (k < 4 ? (u32)(tt * 32 + 4 * w + k) * 1024 : 65536u + (u32)(tt * 8 + w) * 1024) + lane * 16, (u32)s * 81920u);
#pragma unroll
                for (int ct = 0; ct < 4; ++ct)
                    a[k][ct] = k < 4 ? ld4(c.rs, OFF_U + (u32)((4 * cg + ct) * 32 + 4 * w + k) * 1024 + lane * 16, (u32)s * 1310720u)
                                     : ld4(c.rs, OFF_YL + (u32)((4 * cg + ct) * 8 + w) * 1024 + lane * 16, (u32)s * 327680u);
            }
#pragma unroll
            for (int k = 0; k < 5; ++k)
#pragma unroll
                for (int i = 0; i < 8; ++i) acc[i >> 2][i & 3] = mma16(a[k][i & 3], b[k][i >> 2], acc[i >> 2][i & 3]);
            BAR();
#pragma unroll
            for (int i = 0; i < 8; ++i) {
                const float gg = gt[i >> 2];
                res[i >> 2][i & 3] = res[i >> 2][i & 3] * gg + acc[i >> 2][i & 3] * (1.0f - gg);
                *(LAS f32x4*)(c.lds + (w * 8 + i) * 1024 + lane * 16) = res[i >> 2][i & 3];
            }
        }
        BAR();
        f32x4 t = *(LAS f32x4*)(c.lds + w * 1024 + lane * 16);
#pragma unroll
        for (int i = 1; i < 8; ++i) t += *(LAS f32x4*)(c.lds + (i * 8 + w) * 1024 + lane * 16);
        auto e0 = __builtin_amdgcn_permlane32_swap(__float_as_uint(t[0]), __float_as_uint(t[2]), false, false);
        auto e1 = __builtin_amdgcn_permlane32_swap(__float_as_uint(t[1]), __float_as_uint(t[3]), false, false);
        const u32x2 ov = {pk(__uint_as_float(e0[0]), __uint_as_float(e0[1])), pk(__uint_as_float(e1[0]), __uint_as_float(e1[1]))};
        const int tok = 32 * s + 16 * (w >> 2) + n, col = 64 * cg + 16 * (w & 3) + 8 * (g & 1) + 4 * (g >> 1);
        *(GAS u32x2*)(P.out + (size_t)tok * 640 + col) = ov;
    }
}

__global__ void __launch_bounds__(512, 2) mega_kernel(Params P) {
    extern __shared__ __attribute__((aligned(16))) unsigned char smem[];
    Ctx c;
    c.rs = __builtin_amdgcn_make_buffer_rsrc(P.ws, 0, (int)WS_BYTES, 0x00020000);
    c.fl = (gu32*)(P.ws + OFF_FLAG);
    c.tid = threadIdx.x;
    c.wave = __builtin_amdgcn_readfirstlane(threadIdx.x >> 6);
    c.lane = threadIdx.x & 63;
    c.n = c.lane & 15;
    c.g = c.lane >> 4;
    c.lds = (lchar*)smem;
    const int b = blockIdx.x;
    const int idx = (b >> 3) * 4 + (b & 3);
    const int rep = (int)(__builtin_amdgcn_s_getreg(20 | (3 << 11)) & 3u);
    const int ro = (b & 4) == 0 ? idx : 128 + idx;
    if (ro < 32) { if (c.wave < 4) role_q(P, c, ro, rep); else role_k(P, c, ro, rep); DUMP(ro); }
    else if (ro < 64) { role_vom(P, c, ro - 32, rep); DUMP(ro); }
    else if (ro < 104) { role_red(P, c, ro - 64, rep); DUMP(ro); }
    else if (ro < 136) { role_kqx(P, c, ro - 104); DUMP(ro + 88); }
    else if (ro < 168) { role_vox(P, c, ro - 136); DUMP(ro + 88); }
    else {
        {
            const int wk = idx - 40;
            STAMP(104 + wk, 0, 0);
            wrk_qkv(P, c, wk);
            STAMP(104 + wk, 0, 1);
            wrk_barrier(c, F_BAR1, NWRK);
            STAMP(104 + wk, 0, 2);
            wrk_attn(P, c, wk);
            STAMP(104 + wk, 0, 3);
            wrk_barrier(c, F_BAR2, NWRK);
            STAMP(104 + wk, 1, 0);
            wrk_finish(P, c, wk);
            STAMP(104 + wk, 1, 1);
            DUMP(104 + wk);
        }
    }
}

extern "C" void kernel_launch(void* const* d_in, const int* in_sizes, int n_in,
                              void* d_out, int out_size, void* d_ws, size_t ws_size,
                              hipStream_t stream) {
    (void)in_sizes; (void)n_in; (void)out_size; (void)ws_size;
    Params P;
    P.x = (const u16*)d_in[0];
    P.wqkv = (const u16*)d_in[1];
    P.wo = (const u16*)d_in[2];
    P.mem0 = (const u16*)d_in[4];
    P.Wq = (const u16*)d_in[5];
    P.Wk = (const u16*)d_in[6];
    P.Wv = (const u16*)d_in[7];
    P.Wo = (const u16*)d_in[8];
    P.gw = (const u16*)d_in[10];
    P.out = (u16*)d_out;
    P.ws = (unsigned char*)(((uintptr_t)d_ws + (2u * MB - 1)) & ~(uintptr_t)(2u * MB - 1));
    (void)hipFuncSetAttribute((const void*)mega_kernel, hipFuncAttributeMaxDynamicSharedMemorySize, LDS_BYTES);
    (void)hipMemsetAsync(P.ws, 0xFF, ARM_BYTES, stream);
    mega_kernel<<<256, 512, LDS_BYTES, stream>>>(P);
}
```

```cpp
#include <hip/hip_runtime.h>
#include <hip/hip_bf16.h>
#include <stdint.h>

typedef unsigned short u16;
typedef unsigned int u32;
typedef unsigned long long u64;
typedef __bf16 bf16x2 __attribute__((ext_vector_type(2)));
typedef __bf16 bf16x8 __attribute__((ext_vector_type(8)));
typedef __bf16 bf16x16 __attribute__((ext_vector_type(16)));
typedef float f32x2 __attribute__((ext_vector_type(2)));
typedef float f32x4 __attribute__((ext_vector_type(4)));
typedef float f32x16 __attribute__((ext_vector_type(16)));
typedef u32 u32x2 __attribute__((ext_vector_type(2)));
typedef u32 u32x4 __attribute__((ext_vector_type(4)));
typedef u32 u32x8 __attribute__((ext_vector_type(8)));
#define GAS __attribute__((address_space(1)))
#define LAS __attribute__((address_space(3)))
typedef GAS u32 gu32;
typedef LAS unsigned char lchar;
#define RLX_AGENT __ATOMIC_RELAXED, __HIP_MEMORY_SCOPE_AGENT
#define DEV __device__ __forceinline__

constexpr int NSEG = 64;
constexpr u32 SENT = 0xFFFFFFFFu;
constexpr u32 MB = 1u << 20;
constexpr u32 OFF_FLAG = 3 * MB;
constexpr u32 ARM_BYTES = OFF_FLAG + 576 * 256;
constexpr u32 OFF_U = 4 * MB;
constexpr u32 OFF_YL = OFF_U + 80 * MB;
constexpr u32 OFF_KX = OFF_YL + 20 * MB;
constexpr u32 OFF_KM = OFF_KX + 16 * MB;
constexpr u32 OFF_PT = OFF_KM + 4 * MB;
constexpr u32 OFF_Q0 = OFF_PT + 5 * MB;
constexpr u32 OFF_Q1 = OFF_Q0 + 4 * MB;
constexpr u32 OFF_VT = OFF_Q1 + 1 * MB;
constexpr u32 OFF_OA = OFF_VT + 3 * MB;
constexpr u32 OFF_WOF = OFF_OA + 3 * MB;
constexpr u32 OFF_G = OFF_WOF + 1 * MB;
constexpr u32 OFF_RB = OFF_G + 1 * MB;
constexpr u32 OFF_DBG = OFF_RB + 5 * MB;
constexpr u32 WS_BYTES = OFF_DBG + 2 * MB;
constexpr int F_POL = 0, F_BAR1 = 2, F_BAR2 = 3, F_U = 36, F_PT = 68, F_YL = 100, F_TW = 136;
constexpr int F_TK = 512;
constexpr int F_RB = 200;
constexpr int F_KX = 256, F_KM = 384;
constexpr int NWRK = 88, NATT = 64, NFB = NWRK - NATT;
constexpr int S_SPLIT = 48;
constexpr int KQ_LEAD = 4;
constexpr int LDS_BYTES = 160 * 1024;
#define K_PE 44
#define KQ_NAP 6
#define W_AUX 0
#define HELP 0
#define CODEPF 1
#define QK_KD 0
#define T_D 142
#define T_RB 71
#define T_DA 8
#define T_YE 20
#define T_DS 60
#define T_FINE 1
#define T_UD 14
constexpr u32 SPIN_CAP = 1u << 21;
constexpr float LOG2E = 1.4426950408889634f;

struct Params {
    const u16 *x, *wqkv, *wo, *mem0, *Wq, *Wk, *Wv, *Wo, *gw;
    u16* out;
    unsigned char* ws;
};

typedef __amdgpu_buffer_rsrc_t rsrc_t;
DEV u64 rt() { return __builtin_amdgcn_s_memrealtime(); }
DEV u32 pk(float a, float b) { f32x2 v = {a, b}; return __builtin_bit_cast(u32, __builtin_convertvector(v, bf16x2)); }
DEV u32 guard(u32 v) { return v < 0xFFFFFFFEu ? v : 0xFFFFFFFEu; }
DEV float fmx(float a, float b) { return __builtin_elementwise_maximum(a, b); }
DEV u32 umx3(u32 a, u32 b, u32 c) { return max(max(a, b), c); }
DEV u32 umx4(u32x4 v) { return max(umx3(v.x, v.y, v.z), v.w); }
DEV float bf_lo(u32 v) { return __uint_as_float(v << 16); }
DEV float bf_hi(u32 v) { return __uint_as_float(v & 0xFFFF0000u); }
DEV u32x4 lo4(const u32x8& v) { return __builtin_shufflevector(v, v, 0, 1, 2, 3); }
DEV u32x4 hi4(const u32x8& v) { return __builtin_shufflevector(v, v, 4, 5, 6, 7); }
DEV u32x8 cat8(u32x4 a, u32x4 b) { return __builtin_shufflevector(a, b, 0, 1, 2, 3, 4, 5, 6, 7); }
DEV f32x4 mma16(u32x4 a, u32x4 b, f32x4 c) {
    return __builtin_amdgcn_mfma_f32_16x16x32_bf16(__builtin_bit_cast(bf16x8, a), __builtin_bit_cast(bf16x8, b), c, 0, 0, 0); }
DEV f32x16 mma32(u32x4 a, u32x4 b, f32x16 c) {
    return __builtin_amdgcn_mfma_f32_32x32x16_bf16(__builtin_bit_cast(bf16x8, a), __builtin_bit_cast(bf16x8, b), c, 0, 0, 0); }
DEV f32x4 smma(u32x4 a, const u32x8& b, f32x4 c, int idx) {
    return __builtin_amdgcn_smfmac_f32_16x16x64_bf16(__builtin_bit_cast(bf16x8, a), __builtin_bit_cast(bf16x16, b), c, idx, 0, 0); }
DEV float swap_add(float a, float b) {
    auto r = __builtin_amdgcn_permlane32_swap(__float_as_uint(a), __float_as_uint(b), false, false);
    return __uint_as_float(r[0]) + __uint_as_float(r[1]); }
DEV float ror8(float v) { return __uint_as_float(__builtin_amdgcn_update_dpp(0, __float_as_uint(v), 0x128, 0xf, 0xf, false)); }
DEV float max_x16(float v) { auto r = __builtin_amdgcn_permlane16_swap(__float_as_uint(v), __float_as_uint(v), false, false); return fmx(__uint_as_float(r[0]), __uint_as_float(r[1])); }
DEV float max_x32(float v) { auto r = __builtin_amdgcn_permlane32_swap(__float_as_uint(v), __float_as_uint(v), false, false); return fmx(__uint_as_float(r[0]), __uint_as_float(r[1])); }
DEV float add_x16(float v) { auto r = __builtin_amdgcn_permlane16_swap(__float_as_uint(v), __float_as_uint(v), false, false); return __uint_as_float(r[0]) + __uint_as_float(r[1]); }
DEV float add_x32(float v) { auto r = __builtin_amdgcn_permlane32_swap(__float_as_uint(v), __float_as_uint(v), false, false); return __uint_as_float(r[0]) + __uint_as_float(r[1]); }

DEV u32x4 ld4(rsrc_t rs, u32 vo, u32 so) { return __builtin_amdgcn_raw_buffer_load_b128(rs, vo, so, 16); }
DEV u32x2 ld2(rsrc_t rs, u32 vo, u32 so) { return __builtin_amdgcn_raw_buffer_load_b64(rs, vo, so, 16); }
DEV u32 ld1(rsrc_t rs, u32 vo, u32 so) { return __builtin_amdgcn_raw_buffer_load_b32(rs, vo, so, 16); }
DEV void st4(u32x4 v, rsrc_t rs, u32 vo, u32 so) { __builtin_amdgcn_raw_buffer_store_b128(v, rs, vo + so, 0, 16); }
DEV void st2(u32x2 v, rsrc_t rs, u32 vo, u32 so) { __builtin_amdgcn_raw_buffer_store_b64(v, rs, vo, so, 16); }
DEV void st1(u32 v, rsrc_t rs, u32 vo, u32 so) { __builtin_amdgcn_raw_buffer_store_b32(v, rs, vo, so, 16); }
DEV rsrc_t mk_rsrc(const void* p, u32 bytes) { return __builtin_amdgcn_make_buffer_rsrc((void*)p, 0, (int)bytes, 0x00020000); }
DEV u32x4 pld4(rsrc_t rs, u32 vo, u32 so) { return __builtin_amdgcn_raw_buffer_load_b128(rs, vo, so, 0); }
DEV u32x2 pld2(rsrc_t rs, u32 vo, u32 so) { return __builtin_amdgcn_raw_buffer_load_b64(rs, vo, so, 0); }
DEV u32 pld1(rsrc_t rs, u32 vo, u32 so) { return __builtin_amdgcn_raw_buffer_load_b32(rs, vo, so, 0); }
DEV u32 wld1(rsrc_t rs, u32 vo, u32 so) { return __builtin_amdgcn_raw_buffer_load_b32(rs, vo, so, W_AUX); }
DEV u32x4 wld4(rsrc_t rs, u32 vo, u32 so) { return __builtin_amdgcn_raw_buffer_load_b128(rs, vo, so, W_AUX); }
DEV void pst2(u32x2 v, rsrc_t rs, u32 vo, u32 so) { __builtin_amdgcn_raw_buffer_store_b64(v, rs, vo, so, 0); }
DEV u32x4 lds4(lchar* p) { return *(LAS u32x4*)p; }
DEV u32x2 lds2(lchar* p) { return *(LAS u32x2*)p; }
#define BAR() do { asm volatile("s_waitcnt lgkmcnt(0)" ::: "memory"); __builtin_amdgcn_s_barrier(); asm volatile("" ::: "memory"); } while (0)
#define DRAIN() asm volatile("s_waitcnt vmcnt(0)" ::: "memory")
#define FENCE() asm volatile("" ::: "memory")
#define SB() __builtin_amdgcn_sched_barrier(0)
#define PIN(...) asm volatile("" : __VA_ARGS__)

struct Ctx {
    rsrc_t rs;
    gu32* fl;
    int tid, wave, lane, n, g;
    lchar* lds;
};
DEV gu32* flag(const Ctx& c, int i) { return c.fl + i * 64; }
#define SPIN_TICK(cnt) asm volatile("s_sub_u32 %0, %0, 1\n\ts_cbranch_scc0 .Lsp%=\n\ts_endpgm\n.Lsp%=:" : "+s"(cnt) :: "scc", "memory")
template <int NAP = 2>
DEV int wait_ge(const Ctx& c, int fi, int want) {
    u32 sp = SPIN_CAP;
    int v = __builtin_amdgcn_readfirstlane(__hip_atomic_load(flag(c, fi), RLX_AGENT));
    while (v < want) {
        __builtin_amdgcn_s_sleep(NAP);
        SPIN_TICK(sp);
        v = __builtin_amdgcn_readfirstlane(__hip_atomic_load(flag(c, fi), RLX_AGENT));
    }
    return v;
}
DEV int prog4(const Ctx& c, int fi) {
    FENCE();
    int v = (int)__hip_atomic_load(flag(c, fi + (c.lane & 3)), RLX_AGENT);
    v = min(v, __shfl_xor(v, 1)); v = min(v, __shfl_xor(v, 2));
    return __builtin_amdgcn_readfirstlane(v);
}
template <int NAP>
DEV int wait4(const Ctx& c, int fi, int want) {
    u32 sp = SPIN_CAP;
    int v = prog4(c, fi);
    while (v < want) { __builtin_amdgcn_s_sleep(NAP); SPIN_TICK(sp); v = prog4(c, fi); }
    return v;
}
DEV u32 now32() { u64 t; asm volatile("s_memrealtime %0\n\ts_waitcnt lgkmcnt(0)" : "=s"(t) :: "memory"); return (u32)t; }
DEV void wait_until(u32 t) {
    int d = (int)(t - now32());
    if (d <= 0 || d > 1000) return;
    for (int i = 0; i < 400 && d > 3; ++i) d = (int)(t - now32());
    d = d < 0 ? 0 : d;
    if (d & 2) asm volatile("s_nop 10");
    if (d & 1) asm volatile("s_nop 4");
}
DEV u32 read_time(const Ctx& c, int s) {
    u32 v = SENT;
    for (int i = 0; i < 6 && v == SENT; ++i) { FENCE(); v = __builtin_amdgcn_readfirstlane(__hip_atomic_load(c.fl + (F_TW * 64 + s * 32), RLX_AGENT)); }
    return v == SENT ? now32() - 2000u : v;
}
DEV u32 time_word(const Ctx& c, int s) { return __hip_atomic_load(c.fl + (F_TW * 64 + s * 32), RLX_AGENT); }
DEV u32 mem_time(u32 tw) { tw = __builtin_amdgcn_readfirstlane(tw); return tw == SENT ? now32() : tw + (T_RB + T_DA); }
DEV u32 slot_base(int slot, u32 pol) { return (u32)slot * MB + (((pol >> (slot >> 1)) & 1u) << 13); }

#define TSTAMP(t, s, k) do { } while (0)
#define CSTAMP(s, k) do { } while (0)
#define DUMP(role_id) do { } while (0)
#define WSTAMP(k) do { } while (0)
#define STAMP(role_id, s, k) TSTAMP(0, s, k)
#define PDECL do { } while (0)
#define PMARK(k) do { } while (0)
#define PDUMP(role_id) do { } while (0)
DEV u64* smem_dbg(const Ctx& c) { return (u64*)(LAS u64*)(c.lds + LDS_BYTES - 2048); }

template <int NK64, int LDW, bool REV = false>
DEV void load_w(rsrc_t rw, u32 base, int n, int g, u32x8* w0, u32x8* w1) {
    const u32 vo = (u32)(8 * g * LDW + 2 * n) * 2;
#pragma unroll
    for (int kk = 0; kk < NK64; ++kk) {
        const int k = REV ? NK64 - 1 - kk : kk;
        u32 d[16];
#pragma unroll
        for (int j = 0; j < 16; ++j) d[j] = wld1(rw, vo, base + (u32)(64 * k + 32 * (j >> 3) + (j & 7)) * (LDW * 2));
#pragma unroll
        for (int j = 0; j < 8; ++j) {
            w0[k][j] = __builtin_amdgcn_perm(d[2 * j + 1], d[2 * j], 0x05040100);
            w1[k][j] = __builtin_amdgcn_perm(d[2 * j + 1], d[2 * j], 0x07060302);
        }
    }
}

DEV u32 poll_m_regs(const Ctx& c, u32 vo, u32 so, u32x4& v, u32& u) {
    u32 sp = SPIN_CAP;
    do {
        SPIN_TICK(sp);
        v = ld4(c.rs, vo + c.lane * 16, so);
        u = ld1(c.rs, vo + 1024 + c.lane * 4, so);
    } while (__builtin_amdgcn_ballot_w64(umx3(umx3(v.x, v.y, v.z), v.w, u) == SENT) != 0);
    return SPIN_CAP - 1 - sp;
}
DEV u32 poll_m(const Ctx& c, u32 vo, u32 so, lchar* d4, lchar* d1) {
    u32x4 v; u32 u;
    const u32 tries = poll_m_regs(c, vo, so, v, u);
    *(LAS u32x4*)d4 = v;
    *(LAS u32*)d1 = u;
    return tries;
}
DEV void mem_proj(lchar* p, const u32x8* w0, const u32x8* w1, int idx, f32x4 a0, f32x4 a1, u32& oa, u32& ob) {
    u32x4 a[10];
#pragma unroll
    for (int t = 0; t < 5; ++t) a[t] = lds4(p + t * 1024);
    SB();
#pragma unroll
    for (int t = 0; t < 10; ++t) {
        a0 = smma(a[t], w0[t], a0, idx);
        a1 = smma(a[t], w1[t], a1, idx);
        if (t < 5) a[t + 5] = lds4(p + (t + 5) * 1024);
        SB();
    }
    float s0 = swap_add(a0[0], a0[2]), s1 = swap_add(a0[1], a0[3]);
    PIN("+v"(s0), "+v"(s1));
    float s2 = swap_add(a1[0], a1[2]), s3 = swap_add(a1[1], a1[3]);
    PIN("+v"(s2), "+v"(s3));
    oa = pk(s0, s2);
    ob = pk(s1, s3);
}
#define ZERO4 ((f32x4){0.f, 0.f, 0.f, 0.f})
struct XDma { const u16* src; u32 la, vo; };
DEV XDma x_dma(const Ctx& c, const u16* x, int seg, lchar* dst, int rb) {
    XDma d;
    seg = seg < NSEG ? seg : NSEG - 1;
    d.src = x + (32 * seg + 8 * rb) * 640;
    d.la = __builtin_amdgcn_readfirstlane((u32)(uintptr_t)dst + (u32)rb * 10240);
    d.vo = (u32)((c.lane >> 3) * 1280 + ((c.lane & 7) ^ (c.lane >> 3)) * 16);
    return d;
}
template <int K>
DEV void x_piece(const XDma& d) {
    u32 keep;
    asm volatile("s_mov_b32 %0, m0\n\ts_add_u32 m0, %3, %4\n\ts_nop 0\n\tglobal_load_lds_dwordx4 %1, %2 offset:%5\n\ts_mov_b32 m0, %0"
                 : "=&s"(keep) : "v"(d.vo), "s"(d.src), "s"(d.la), "i"(K * 896), "i"(K * 128) : "memory", "scc");
}
DEV void x_piece_rt(const XDma& d, int k) {
    switch (k) { case 0: x_piece<0>(d); break; case 1: x_piece<1>(d); break; case 2: x_piece<2>(d); break; case 3: x_piece<3>(d); break;
                 case 4: x_piece<4>(d); break; case 5: x_piece<5>(d); break; case 6: x_piece<6>(d); break; case 7: x_piece<7>(d); break;
                 case 8: x_piece<8>(d); break; default: x_piece<9>(d); break; }
}
DEV void x_stage(const Ctx& c, const u16* x, int seg, lchar* dst, int rb) {
    const XDma d = x_dma(c, x, seg, dst, rb);
#pragma unroll
    for (int k = 0; k < 10; ++k) x_piece_rt(d, k);
}
DEV int x_lane_off(const Ctx& c) { return (c.n >> 3) * 10240 + (c.n & 7) * 128 + ((c.g ^ (c.n & 7)) * 16); }
DEV u32x4 x_frag(lchar* pe, lchar* po, int tt, int ks) { return lds4(((ks & 1) ? po : pe) + tt * 20480 + (ks >> 1) * 1024); }
template <bool DMA, bool FLAG, bool SWAP>
DEV void tok_acc(const Ctx& c, lchar* ximg, const u32x8* w0, const u32x8* w1, f32x4 (&acc)[2][2], const XDma& dm, gu32* fl, u32 fv) {
#pragma unroll
    for (int i = 0; i < 4; ++i) acc[i >> 1][i & 1] = (f32x4){0.f, 0.f, 0.f, 0.f};
    const int lo = x_lane_off(c);
    lchar* pe = ximg + lo;
    lchar* po = ximg + (lo ^ 64);
    u32x4 b[20][2];
#pragma unroll
    for (int ks = 0; ks < 3; ++ks) { b[ks][0] = x_frag(pe, po, 0, ks); b[ks][1] = x_frag(pe, po, 1, ks); }
    SB();
#pragma unroll
    for (int ks = 0; ks < 20; ++ks) {
        const u32x4 a0 = (ks & 1) ? hi4(w0[ks >> 1]) : lo4(w0[ks >> 1]);
        const u32x4 a1 = (ks & 1) ? hi4(w1[ks >> 1]) : lo4(w1[ks >> 1]);
#pragma unroll
        for (int tt = 0; tt < 2; ++tt) {
            acc[tt][0] = SWAP ? mma16(b[ks][tt], a0, acc[tt][0]) : mma16(a0, b[ks][tt], acc[tt][0]);
            acc[tt][1] = SWAP ? mma16(b[ks][tt], a1, acc[tt][1]) : mma16(a1, b[ks][tt], acc[tt][1]);
        }
        SB();
        if (ks + 3 < 20) { b[ks + 3][0] = x_frag(pe, po, 0, ks + 3); b[ks + 3][1] = x_frag(pe, po, 1, ks + 3); }
        if (DMA && ks < 10) x_piece_rt(dm, ks);
        if (FLAG && ks == 5) { DRAIN(); __hip_atomic_store(fl, fv, RLX_AGENT); }
        SB();
    }
}
template <bool DMA>
DEV void tok_proj(const Ctx& c, lchar* ximg, const u32x8* w0, const u32x8* w1, u32x4 (&o)[2], const XDma& dm, gu32* fl = nullptr, u32 fv = 0) {
    f32x4 acc[2][2];
    tok_acc<DMA, !DMA, false>(c, ximg, w0, w1, acc, dm, fl, fv);
#pragma unroll
    for (int tt = 0; tt < 2; ++tt)
#pragma unroll
        for (int r = 0; r < 4; ++r) o[tt][r] = pk(acc[tt][0][r], acc[tt][1][r]);
}

DEV void warm_rows(const Ctx& c, const u16* W, u32 base, u32 ld, int r0, int r1) {
    const rsrc_t rw = mk_rsrc(W, 640 * 4096 * 2);
    u32 acc = 0;
#pragma unroll 5
    for (int r = r1 - 32; r >= r0; r -= 32) acc ^= wld4(rw, (u32)(r + (c.tid >> 4)) * ld + (c.tid & 15) * 16, base).x;
    asm volatile("" :: "v"(acc));
}
DEV void warm_head(const Params& P, const Ctx& c, bool vo, int h, int part, int parts, bool low) {
    const int n = 320 / parts, r0 = (low ? 0 : 320) + part * n;
    warm_rows(c, vo ? P.Wv : P.Wq, (u32)h * 256, 8192, r0, r0 + n);
    if (vo) warm_rows(c, P.Wo, (u32)h * 163840, 256, r0, r0 + n);
    else warm_rows(c, P.Wk, (u32)h * 256, 8192, r0, r0 + n);
}

DEV u32 qk_p_vo(const Ctx& c, int h) {
    const int w = c.wave & 3, n = c.n, g = c.g;
    return (g & 1) == 0 ? (u32)((8 + 3 * w + (h >> 4)) * 16384 + ((h >> 1) & 7) * 1024 + ((2 * (h & 1) + (g >> 1)) * 16 + n) * 16)
                        : (c.lane < 24 ? (u32)((8 + 3 * w + 2) * 16384 + h * 128 + n * 16) : 0x80000000u);
}
DEV void role_q(const Params& P, const Ctx& c, int h, int rep) {
    const int w = c.wave, n = c.n, g = c.g, lane = c.lane;
    u32x8 w0[10], w1[10];
    load_w<10, 4096>(mk_rsrc(P.Wq, 640 * 4096 * 2), (u32)(h * 128 + 32 * w) * 2, n, g, w0, w1);
    const u32 pol = (u32)wait_ge(c, F_POL, 0);
    lchar* qs = c.lds + 10240;
    const int idx = n < 8 ? 0x4444 : 0xEEEE;
    const u32 m_vo = (u32)(2 * rep * 16384 + w * 1280);
    lchar* q_wr = qs + w * 576 + (n >> 2) * 144 + (4 * (g & 1) + 2 * (g >> 1)) * 16 + (n & 3) * 4;
    lchar* q_rd = qs + g * 144 + (n & 7) * 16;
    const int pr = (n & 1) | ((n & 2) << 1) | ((n & 4) >> 1);
    lchar* k_rd = qs + 2304 + g * 144 + pr * 16;
    const u32 p_vo = qk_p_vo(c, h);
    const float cs = 0.08838834764831845f * LOG2E;
    u32x4 kx[8];
    wait4<2>(c, F_KX + 4 * h, 0);
#pragma unroll
    for (int i = 0; i < 8; ++i) kx[i] = ld4(c.rs, OFF_KX + (u32)(h * 8 + i) * 1024 + lane * 16, 0);
    u32 t_m = now32();
    lchar* mrd = c.lds + lane * 16; lchar* md4 = mrd + w * 1280; lchar* md1 = c.lds + w * 1280 + 1024 + lane * 4;
    volatile LAS u32* qcnt = (volatile LAS u32*)(c.lds + 15360);
    if (c.tid < 8) qcnt[c.tid] = 0;
    int slot = 0;
    PDECL;
    for (int s = 0; s < NSEG; ++s) {
        const u32 sb = slot_base(slot, pol);
        f32x4 z0 = ZERO4, z1 = ZERO4;
        u32 p_cur = p_vo + sb;
        PIN("+v"(z0), "+v"(z1), "+v"(md4), "+v"(md1), "+v"(mrd), "+v"(p_cur));
        wait_until(t_m);
        TSTAMP(0, s, 2);
        const u32 tries = poll_m(c, m_vo, sb, md4, md1);
        PMARK(0);
        BAR();
        if (s == NSEG - 1) break;
        PMARK(1);
        STAMP(h, s, 0);
        u32 oa, ob;
        mem_proj(mrd, w0, w1, idx, z0, z1, oa, ob);
        *(LAS u32*)(q_wr) = oa;
        *(LAS u32*)(q_wr + 16) = ob;
        FENCE();
        qcnt[w] = (u32)(s + 1);
        PMARK(6);
        u32x4 q[4], km[4];
        u32 got, sp = SPIN_CAP;
        do {
            SPIN_TICK(sp);
            got = qcnt[lane & 3];
#pragma unroll
            for (int ks = 0; ks < 4; ++ks) q[ks] = *(volatile LAS u32x4*)(q_rd + ks * 576);
        } while (__builtin_amdgcn_ballot_w64(got != (u32)(s + 1)) != 0);
        PMARK(2);
        f32x4 d0 = ZERO4, d1 = ZERO4, d2 = ZERO4;
#pragma unroll
        for (int ks = 0; ks < 4; ++ks) {
            d0 = mma16(kx[ks], q[ks], d0);
            d1 = mma16(kx[4 + ks], q[ks], d1);
        }
        PIN("+v"(d0), "+v"(d1));
        BAR();
        PMARK(3);
#pragma unroll
        for (int ks = 0; ks < 4; ++ks) km[ks] = lds4(k_rd + ks * 576);
        SB();
        float v[4], e[4];
#pragma unroll
        for (int r = 0; r < 4; ++r) v[r] = n < 8 ? d0[r] : d1[r];
        float mx = fmx(fmx(fmx(v[0], v[1]), v[2]), v[3]);
        mx = fmx(mx, ror8(mx)); mx = max_x16(mx); mx = max_x32(mx);
        float mxc = mx * cs;
        PIN("+v"(mxc), "+v"(km[0]));
        SB();
#pragma unroll
        for (int ks = 0; ks < 4; ++ks) d2 = mma16(km[ks], q[ks], d2);
        SB();
#pragma unroll
        for (int r = 0; r < 4; ++r) v[r] = __builtin_amdgcn_exp2f(__builtin_fmaf(v[r], cs, -mxc));
        float sx = (v[0] + v[1]) + (v[2] + v[3]);
        sx += ror8(sx); sx = add_x16(sx); sx = add_x32(sx);
        PIN("+v"(sx), "+v"(d2));
        PMARK(4);
        const float lm = fmx(fmx(fmx(d2[0], d2[1]), d2[2]), d2[3]);
        const float mc = fmx(max_x16(lm), mx) * cs;
        const float fx = __builtin_amdgcn_exp2f(mxc - mc);
#pragma unroll
        for (int r = 0; r < 4; ++r) e[r] = __builtin_amdgcn_exp2f(__builtin_fmaf(d2[r], cs, -mc));
        const float inv = __builtin_amdgcn_rcpf(__builtin_fmaf(sx, fx, add_x16((e[0] + e[1]) + (e[2] + e[3]))));
        const float ix = inv * fx;
        u32 o0, o1, o2, o3;
        asm("v_cvt_pk_bf16_f32 %0, %4, |%5|\n\tv_cvt_pk_bf16_f32 %2, %8, |%9|\n\tv_cvt_pk_bf16_f32 %1, %6, |%7|\n\tv_cvt_pk_bf16_f32 %3, %10, |%11|\n\t"
            "v_permlane16_swap_b32 %0, %2\n\ts_nop 0\n\tv_permlane16_swap_b32 %1, %3"
            : "=&v"(o0), "=&v"(o1), "=&v"(o2), "=&v"(o3)
            : "v"(v[0] * ix), "v"(v[1] * ix), "v"(v[2] * ix), "v"(v[3] * ix), "v"(e[0] * inv), "v"(e[1] * inv), "v"(e[2] * inv), "v"(e[3] * inv));
        st4((u32x4){o0, o1, o2, o3}, c.rs, p_cur, 0);
        STAMP(h, s, 1);
        PMARK(5);
        do {
            SPIN_TICK(sp);
            got = qcnt[4 + (lane & 3)];
#pragma unroll
            for (int i = 0; i < 8; ++i) kx[i] = *(volatile LAS u32x4*)(mrd + 16384 + i * 1024);
        } while (__builtin_amdgcn_ballot_w64(got != (u32)(s + 1)) != 0);
        t_m = mem_time(qcnt[8]);
        slot = slot == 2 ? 0 : slot + 1;
    }
    asm volatile(".Lmk_tail:");
    PDUMP(h);
}
DEV void role_k(const Params& P, const Ctx& c, int h, int rep) {
    const int w = c.wave & 3, n = c.n, g = c.g, lane = c.lane;
    u32x8 w0[10], w1[10];
    load_w<10, 4096>(mk_rsrc(P.Wk, 640 * 4096 * 2), (u32)(h * 128 + 32 * w) * 2, n, g, w0, w1);
    const u32 pol = (u32)wait_ge(c, F_POL, 0);
    lchar* ks_ = c.lds + 10240 + 2304;
    const int idx = n < 8 ? 0x4444 : 0xEEEE;
    const u32 m_vo = (u32)((2 * rep + 1) * 16384 + w * 1280);
    lchar* k_wr = ks_ + w * 576 + (n >> 2) * 144 + (4 * (g & 1) + 2 * (g >> 1)) * 16 + (n & 3) * 4;
    const u32 p_vo = qk_p_vo(c, h);
    const u32 kx_vo = OFF_KX + (u32)(h * 8 + 2 * w) * 1024 + lane * 16;
    const u32 km_vo = lane < 32 ? OFF_KM + (u32)(h * 4 + w) * 512 + lane * 16 : 0x80000000u;
    lchar* km_rd = ks_ + w * 576 + ((lane >> 3) & 3) * 144 + (lane & 7) * 16;
    lchar* mrd = c.lds + lane * 16; lchar* md4 = mrd + (4 + w) * 1280; lchar* md1 = c.lds + (4 + w) * 1280 + 1024 + lane * 4;
    volatile LAS u32* qcnt = (volatile LAS u32*)(c.lds + 15360);
    gu32* f_km = flag(c, F_KM + 4 * h + w);
    int kprog = -1, slot = 0;
    u32 t_m = now32();
    PDECL;
    for (int s = 0; s < NSEG; ++s) {
        const u32 sb = slot_base(slot, pol);
        f32x4 z0 = ZERO4, z1 = ZERO4;
        u32 p_old = p_vo + slot_base(slot == 0 ? 2 : slot - 1, pol);
        PIN("+v"(z0), "+v"(z1), "+v"(md4), "+v"(md1), "+v"(mrd), "+v"(p_old));
        wait_until(t_m);
        poll_m(c, m_vo, sb, md4, md1);
        PMARK(0);
        BAR();
        PMARK(1);
        st4((u32x4){SENT, SENT, SENT, SENT}, c.rs, p_old, 0);
        __hip_atomic_store(f_km, (u32)(s - 1), RLX_AGENT);
        const bool kget = kprog > s;
        u32x4 k0, k1;
        if (kget) { k0 = ld4(c.rs, kx_vo, (u32)(s + 1) * 262144u); k1 = ld4(c.rs, kx_vo + 1024, (u32)(s + 1) * 262144u); }
        const u32 tw = time_word(c, s);
        __builtin_amdgcn_s_sleep(QK_KD);
        SB();
        u32 oa, ob;
        mem_proj(mrd, w0, w1, idx, z0, z1, oa, ob);
        *(LAS u32*)(k_wr) = oa;
        *(LAS u32*)(k_wr + 16) = ob;
        PMARK(2);
        if (s < NSEG - 1) BAR();
        st4(lds4(km_rd), c.rs, km_vo, (u32)s * 65536u);
        if (s == NSEG - 1) { DRAIN(); __hip_atomic_store(f_km, (u32)s, RLX_AGENT); break; }
        if (!kget && s + 1 < NSEG - 1) {
            wait4<2>(c, F_KX + 4 * h, s + 1);
            k0 = ld4(c.rs, kx_vo, (u32)(s + 1) * 262144u); k1 = ld4(c.rs, kx_vo + 1024, (u32)(s + 1) * 262144u);
        }
        *(LAS u32x4*)(mrd + 16384 + w * 2048) = k0;
        *(LAS u32x4*)(mrd + 16384 + w * 2048 + 1024) = k1;
        if (w == 0) qcnt[8] = tw;
        FENCE();
        qcnt[4 + w] = (u32)(s + 1);
        t_m = mem_time(tw);
        wait_until(t_m - K_PE);
        kprog = prog4(c, F_KX + 4 * h);
        slot = slot == 2 ? 0 : slot + 1;
    }
    PDUMP(h);
}

DEV void load_vo(const Params& P, const Ctx& c, int h, u32x8* wf) {
    if (c.wave < 4) load_w<10, 4096>(mk_rsrc(P.Wv, 640 * 4096 * 2), (u32)(h * 128 + 32 * c.wave) * 2, c.n, c.g, wf, wf + 10);
    else {
#pragma unroll
        for (int q = 0; q < 10; ++q) {
            const int k = q / 5, p = q % 5;
            load_w<1, 640>(mk_rsrc(P.Wo, 640 * 4096 * 2), (u32)((h * 128 + 64 * k) * 640 + 160 * (c.wave - 4) + 32 * p) * 2, c.n, c.g, wf + 4 * p + k, wf + 4 * p + 2 + k);
        }
    }
}
DEV void y_log(const Ctx& c, const u32x2 (&y)[5], u32 vo, u32 so) {
#pragma unroll
    for (int p = 0; p < 5; p += 2) {
        const u32x2 b = y[p < 4 ? p + 1 : p];
        auto r0 = __builtin_amdgcn_permlane16_swap(y[p].x, b.x, false, false);
        auto r1 = __builtin_amdgcn_permlane16_swap(y[p].y, b.y, false, false);
        st4((u32x4){r0[0], r1[0], r0[1], r1[1]}, c.rs, p == 4 && (c.g & 1) ? 0x80000000u : vo + p * 16384, so);
    }
}
DEV void role_vom(const Params& P, const Ctx& c, int h, int rep) {
    const int w = c.wave, n = c.n, g = c.g, lane = c.lane;
    u32x8 wf[20];
    if (w < 4) load_w<10, 4096>(mk_rsrc(P.Wv, 640 * 4096 * 2), (u32)(h * 128 + 32 * w) * 2, n, g, wf, wf + 10);
    else {
        const rsrc_t rw = mk_rsrc(P.Wo, 640 * 4096 * 2);
        const u32 vo = (u32)(4 * g * 640 + 2 * n) * 2;
#pragma unroll
        for (int qq = 0; qq < 20; ++qq) {
            const int q = 4 * (qq % 5) + qq / 5;
            u32 d[8];
#pragma unroll
            for (int j = 0; j < 8; ++j)
                d[j] = wld1(rw, vo, (u32)((h * 128 + 32 * (q & 3) + 16 * (j >> 2) + (j & 3)) * 640 + 160 * (w - 4) + 32 * (q >> 2)) * 2);
#pragma unroll
            for (int u = 0; u < 4; ++u) {
                wf[q][2 * u] = __builtin_amdgcn_perm(d[2 * u + 1], d[2 * u], 0x05040100);
                wf[q][2 * u + 1] = __builtin_amdgcn_perm(d[2 * u + 1], d[2 * u], 0x07060302);
            }
        }
    }
    const u32 pol = (u32)wait_ge(c, F_POL, 0);
    lchar* mimg = c.lds;
    lchar* vs = c.lds + 10240;
    const int idx = n < 8 ? 0x4444 : 0xEEEE;
    const u32 m_vo = (u32)((2 * rep + (w >> 2)) * 16384 + (w & 3) * 1280);
    lchar* v_wr = vs + (w & 3) * 576 + (n >> 2) * 144 + (4 * (g & 1) + 2 * (g >> 1)) * 16 + (n & 3) * 4;
    lchar* v_rd = vs + g * 144 + (n & 7) * 16;
    lchar* md4 = mimg + w * 1280 + lane * 16; lchar* md1 = mimg + w * 1280 + 1024 + lane * 4; lchar* mrd = mimg + lane * 16;
    const int r0 = 10 * (w & 3) + (n >> 3);
    const u32 inner = (u32)(h * 256 + (8 * (g >> 1) + (n & 7)) * 16 + (g & 1) * 8);
    const u32 y_vo = (u32)(20 + r0) * 16384 + inner;
    const u32 yl_vo = OFF_YL + (u32)(r0 + 2 * (g & 1)) * 8192 + (inner & ~8u);
    const u32 ya_vo = (u32)(20 + 10 * (w & 3) + g) * 16384 + (u32)(h * 256 + n * 16);
    int slot = 0;
    u32 t_m = now32();
    PDECL;
    if (w < 4) {
        for (int s = 0; s < NSEG; ++s) {
            const u32 sb = slot_base(slot, pol);
            f32x4 z0 = ZERO4, z1 = ZERO4;
            PIN("+v"(z0), "+v"(z1), "+v"(md4), "+v"(md1), "+v"(mrd));
            wait_until(t_m);
            poll_m(c, m_vo, sb, md4, md1);
            PMARK(0);
            BAR();
            PMARK(1);
            STAMP(32 + h, s, 0);
            u32 oa, ob;
            mem_proj(mrd, wf, wf + 10, idx, z0, z1, oa, ob);
            *(LAS u32*)(v_wr) = oa;
            *(LAS u32*)(v_wr + 16) = ob;
            PMARK(2);
            BAR();
            t_m = mem_time(time_word(c, s));
            slot = slot == 2 ? 0 : slot + 1;
        }
    } else {
        u32x2 y[5];
        for (int s = 0; s < NSEG; ++s) {
            const u32 sb = slot_base(slot, pol);
            f32x4 d[5];
#pragma unroll
            for (int p = 0; p < 5; ++p) d[p] = ZERO4;
            PIN("+v"(d[0]), "+v"(d[1]), "+v"(d[2]), "+v"(d[3]), "+v"(d[4]), "+v"(md4), "+v"(md1));
            wait_until(t_m);
            poll_m(c, m_vo, sb, md4, md1);
            PMARK(0);
            BAR();
            PMARK(1);
            const u32 tw = time_word(c, s);
            if (w == 5 && s > 1) __hip_atomic_store(flag(c, F_YL + h), (u32)(s - 2), RLX_AGENT);
            if (s > 0) {
                y_log(c, y, yl_vo, (u32)(s - 1) * 327680u);
                const u32 sb2 = slot_base(slot == 0 ? 2 : slot - 1, pol);
#pragma unroll
                for (int p = 0; p < 3; ++p) st4((u32x4){SENT, SENT, SENT, SENT}, c.rs, p == 2 && g >= 2 ? 0x80000000u : ya_vo + p * 65536, sb2);
            }
            BAR();
            PMARK(3);
            u32x4 a[4];
#pragma unroll
            for (int ks = 0; ks < 4; ++ks) a[ks] = lds4(v_rd + ks * 576);
            SB();
#pragma unroll
            for (int p = 0; p <= 5; ++p) {
                if (p < 5) {
                    d[p] = smma(a[0], wf[4 * p], d[p], idx);
                    d[p] = smma(a[1], wf[4 * p + 1], d[p], idx);
                    d[p] = smma(a[2], wf[4 * p + 2], d[p], idx);
                    SB();
                }
                if (p > 0) {
                    y[p - 1] = (u32x2){guard(pk(d[p - 1][0], d[p - 1][1])), guard(pk(d[p - 1][2], d[p - 1][3]))};
                    st2(y[p - 1], c.rs, y_vo + (p - 1) * 32768, sb);
                    SB();
                }
                if (p < 5) { d[p] = smma(a[3], wf[4 * p + 3], d[p], idx); SB(); }
            }
            TSTAMP(256, s, 2);
            PMARK(4);
            t_m = mem_time(tw);
            slot = slot == 2 ? 0 : slot + 1;
        }
        y_log(c, y, yl_vo, (u32)(NSEG - 1) * 327680u);
    }
    DRAIN();
    BAR();
    if (c.tid == 0) __hip_atomic_store(flag(c, F_YL + h), (u32)(NSEG - 1), RLX_AGENT);
    PDUMP(32 + h);
}

DEV u32 probe(const Ctx& c, u32 off) {
    u64 a, b; u32 v;
    asm volatile("s_memtime %0\n\ts_waitcnt lgkmcnt(0)" : "=s"(a) :: "memory");
    v = ld1(c.rs, off, 0);
    asm volatile("s_waitcnt vmcnt(0)" : "+v"(v) :: "memory");
    asm volatile("s_memtime %0\n\ts_waitcnt lgkmcnt(0)" : "=s"(b) :: "memory");
    return (u32)(b - a);
}
DEV void role_red(const Params& P, const Ctx& c, int r, int rep) {
    const int w = c.wave, n = c.n, g = c.g, lane = c.lane;
    if (r == 0) {
        if (lane < 32) st1(0u, c.rs, (u32)(lane >> 4) * 2 * MB + 62 * 16384 + ((lane >> 3) & 1) * 8192 + (u32)(8 * w + (lane & 7)) * 128, 0);
        DRAIN();
        u32 best[4] = {~0u, ~0u, ~0u, ~0u};
        for (int i = 0; i < 4; ++i) {
#pragma unroll
            for (int k = 0; k < 4; ++k)
                best[k] = min(best[k], probe(c, (u32)(k >> 1) * 2 * MB + 62 * 16384 + (k & 1) * 8192 + (u32)(8 * w + i) * 128));
        }
        LAS u32* bs = (LAS u32*)(c.lds + 16384);
        if (lane == 0) *(LAS u32x4*)(bs + 4 * w) = (u32x4){best[0], best[1], best[2], best[3]};
        BAR();
        if (c.tid == 0) {
            for (int i = 1; i < 8; ++i)
                for (int k = 0; k < 4; ++k) best[k] = min(best[k], bs[4 * i + k]);
            __hip_atomic_store(flag(c, F_POL), (best[1] < best[0] ? 1u : 0u) | (best[3] < best[2] ? 2u : 0u), RLX_AGENT);
        }
    }
    if (HELP && r > 0) warm_head(P, c, r < 32, r < 32 ? r : r - 8, 0, 1, false);
    const u32 pol = (u32)wait_ge(c, F_POL, 0);
    const int idx = n < 8 ? 0x4444 : 0xEEEE;
    const int oi = (lane >> 2) & 7, oq = 2 * (lane & 3) + (lane >> 5);
    const u32 mo = (u32)(r * 256 + lane * 4);
    const u32 m_vo = (u32)((2 * (w & 3) + (r >= 20)) * 16384) + mo - (r >= 20 ? 5120u : 0u);
    if (w < 4) {
        const u32 v = *(const GAS u32*)(P.mem0 + oi * 640 + 16 * r + 2 * oq);
        st1(guard(v), c.rs, m_vo, slot_base(0, pol));
    }
    lchar* r_wr = c.lds + ((4 * (g & 1) + 2 * (g >> 1) + 8 * (n & 1)) * 4 + ((n & 7) >> 1)) * 80 + w * 8 + (n >> 3) * 4;
    lchar* r_rd = c.lds + lane * 80;
    const u32 y_vo = (u32)((20 + r) * 16384 + w * 1024 + lane * 16);
    const u32 rot = (u32)(r >> 2) & 1u;
    const u32 px_vo = (u32)((8 + 3 * rep + (w >> 2)) * 16384 + (w & 3) * 2048 + lane * 16) + rot * 1024, px_vo1 = px_vo ^ 1024u;
    const u32 pm_vo = lane < 32 ? (u32)((8 + 3 * rep + 2) * 16384 + (4 * w + 2 * g) * 128 + (n & 7) * 16 + (n >> 3) * 8) : 0x80000000u;
    const u32 u_vo = OFF_U + (u32)(r * 32 + 4 * w) * 1024 + lane * 16 + rot * 2048, u_vo1 = u_vo ^ 2048u;
    int uprog = -1;
    PDECL;
    int slot = 0;
    u32 t_p = now32() - 2000u;
    for (int s = 0; s < NSEG - 1; ++s) {
        u32 sp = SPIN_CAP;
        const u32 sb = slot_base(slot, pol);
        const int slotn = slot == 2 ? 0 : slot + 1;
        __builtin_amdgcn_s_sleep(T_UD);
        while (uprog < s) { SPIN_TICK(sp); uprog = prog4(c, F_U + 4 * w); }
        const u32 ub = (u32)s * 1310720u;
        const u32x8 u0 = cat8(ld4(c.rs, u_vo, ub), ld4(c.rs, u_vo + 1024, ub));
        const u32x8 u1 = cat8(ld4(c.rs, u_vo1, ub), ld4(c.rs, u_vo1 + 1024, ub));
        if (s > 0) {
            if (w < 4) st1(SENT, c.rs, m_vo, slot_base(slot == 0 ? 2 : slot - 1, pol));
            t_p = read_time(c, s);
        }
        if (uprog == s) uprog = prog4(c, F_U + 4 * w);
        const u32 m_so = slot_base(slotn, pol);
        const int par = (s & 1) * 5120;
        u32x4 y, p0, p1; u32x2 ma, mb;
        f32x4 d = ZERO4;
        u32x4 zq = {0u, 0u, 0u, 0u};
        lchar* rw_ = r_wr + par; lchar* rr_ = r_rd + par;
        PIN("+v"(d), "+v"(zq), "+v"(rw_), "+v"(rr_));
        wait_until(t_p - T_YE);
        y = ld4(c.rs, y_vo, sb);
        wait_until(t_p);
        TSTAMP(0, s, 2);
        PMARK(1);
        for (;;) {
            FENCE();
            p0 = ld4(c.rs, px_vo, sb);
            p1 = ld4(c.rs, px_vo1, sb);
            ma = ld2(c.rs, pm_vo, sb);
            mb = ld2(c.rs, pm_vo + 128, sb);
            const u32 mxv = umx3(umx3(umx4(y), umx4(p0), umx4(p1)), umx3(ma.x, ma.y, mb.x), mb.y);
            if (__builtin_expect(__builtin_amdgcn_ballot_w64(mxv == SENT) == 0, 1)) break;
            SPIN_TICK(sp);
            y = ld4(c.rs, y_vo, sb);
        }
        PMARK(2);
        d = smma(p0, u0, d, idx);
        d = smma(p1, u1, d, idx);
        d = smma((u32x4){ma.x, ma.y, mb.x, mb.y}, cat8(y, zq), d, idx);
        *(LAS float*)(rw_) = swap_add(d[0], d[2]);
        *(LAS float*)(rw_ + 320) = swap_add(d[1], d[3]);
        PMARK(3);
        BAR();
        PMARK(4);
        STAMP(64 + r, s, 0);
        if (w < 4) {
            f32x4 a[4];
#pragma unroll
            for (int i = 0; i < 4; ++i) a[i] = *(LAS f32x4*)(rr_ + i * 16);
            const f32x4 t = (a[0] + a[1]) + (a[2] + a[3]);
            st1(guard(pk(t[0] + t[2], t[1] + t[3])), c.rs, m_vo, m_so);
            STAMP(64 + r, s, 1);
            PMARK(5);
        } else if (r == 0 && w == 5) {
            const u32 t = now32(), due = t_p + T_RB;
            const u32 nx = (int)(t - due) <= 0 ? due + T_D : t + (T_D + T_DS);
            if (lane == 0) __hip_atomic_store(c.fl + (F_TW * 64 + (s + 1) * 32), guard(nx), RLX_AGENT);
        }
        slot = slotn;
    }
    PDUMP(64 + r);
}

DEV void role_kqx(const Params& P, const Ctx& c, int h) {
    const int w = c.wave, n = c.n, g = c.g, lane = c.lane;
    u32x8 w0[10], w1[10];
    if (w < 4) x_stage(c, P.x, 0, c.lds, w);
    load_w<10, 4096, true>(mk_rsrc(w < 4 ? P.Wq : P.Wk, 640 * 4096 * 2), (u32)(h * 128 + 32 * (w & 3)) * 2, n, g, w0, w1);
    lchar* ximg = c.lds;
    lchar* ring = c.lds + 81920;
    const float cs = 0.08838834764831845f * LOG2E;
    const u32 km_vo = OFF_KM + (u32)h * 2048 + (u32)(g * 8 + (n & 7)) * 16;
    int mprog = -1;
    DRAIN();
    BAR();
    int rw = 0;
    for (int i = -KQ_LEAD; i < NSEG; ++i) {
        const int j = i + KQ_LEAD;
        u32x4 km[4];
        const bool early = w < 2 && i >= 0 && mprog >= i;
        if (early) {
#pragma unroll
            for (int ks = 0; ks < 4; ++ks) km[ks] = ld4(c.rs, km_vo + ks * 512, (u32)i * 65536u);
        }
        if (j < NSEG) {
            u32x4 o[2];
            const XDma dm = x_dma(c, P.x, j + 1, ximg + ((j + 1) & 1) * 40960, w & 3);
            if (w < 4) tok_proj<true>(c, ximg + (j & 1) * 40960, w0, w1, o, dm);
            else tok_proj<false>(c, ximg + (j & 1) * 40960, w0, w1, o, dm, flag(c, F_KX + 4 * h + (w & 3)), (u32)(j - 1));
#pragma unroll
            for (int tt = 0; tt < 2; ++tt) {
                *(LAS u32x4*)(ring + rw * 16384 + (w >> 2) * 8192 + (tt * 4 + (w & 3)) * 1024 + lane * 16) = o[tt];
                if (w >= 4) st4(o[tt], c.rs, OFF_KX + (u32)(h * 8 + tt * 4 + (w & 3)) * 1024 + lane * 16, (u32)j * 262144u);
            }
        }
        rw = rw == KQ_LEAD ? 0 : rw + 1;
        if (w < 2 && i >= 0) {
            f32x4 d0 = ZERO4, d1 = ZERO4, d2 = ZERO4;
            u32x4 qf[4];
#pragma unroll
            for (int ks = 0; ks < 4; ++ks) {
                qf[ks] = lds4(ring + rw * 16384 + (w * 4 + ks) * 1024 + lane * 16);
                d0 = mma16(lds4(ring + rw * 16384 + 8192 + ks * 1024 + lane * 16), qf[ks], d0);
                d1 = mma16(lds4(ring + rw * 16384 + 12288 + ks * 1024 + lane * 16), qf[ks], d1);
            }
            if (!early) {
                mprog = wait4<KQ_NAP>(c, F_KM + 4 * h, i);
#pragma unroll
                for (int ks = 0; ks < 4; ++ks) km[ks] = ld4(c.rs, km_vo + ks * 512, (u32)i * 65536u);
            }
#pragma unroll
            for (int ks = 0; ks < 4; ++ks) d2 = mma16(km[ks], qf[ks], d2);
            DRAIN();
            const float NEG = -1.0e9f / 0.08838834764831845f;
            const int a = 16 * w + n;
            float v[12];
#pragma unroll
            for (int r = 0; r < 4; ++r) {
                v[r] = (4 * g + r <= a) ? d0[r] : NEG;
                v[4 + r] = (16 + 4 * g + r <= a) ? d1[r] : NEG;
                v[8 + r] = g < 2 ? d2[r] : -INFINITY;
            }
            float mx = v[0];
#pragma unroll
            for (int r = 1; r < 12; ++r) mx = fmx(mx, v[r]);
            mx = max_x16(mx); mx = max_x32(mx);
            const float mc = -mx * cs;
            float sum = 0.f;
#pragma unroll
            for (int r = 0; r < 12; ++r) { v[r] = __builtin_amdgcn_exp2f(__builtin_fmaf(v[r], cs, mc)); sum += v[r]; }
            sum = add_x16(sum); sum = add_x32(sum);
            const float inv = __builtin_amdgcn_rcpf(sum);
            const u32 pb = (u32)i * 81920u;
            st4((u32x4){pk(v[0] * inv, v[1] * inv), pk(v[4] * inv, v[5] * inv), pk(v[2] * inv, v[3] * inv), pk(v[6] * inv, v[7] * inv)},
                c.rs, OFF_PT + (u32)(w * 32 + h) * 1024 + lane * 16, pb);
            st2((u32x2){pk(v[8] * inv, v[9] * inv), pk(v[10] * inv, v[11] * inv)}, c.rs,
                g < 2 ? OFF_PT + 65536u + (u32)(w * 8 + (h >> 2)) * 1024 + (u32)((h & 3) * 16 + n) * 16 + g * 8 : 0x80000000u, pb);
        } else if (w < 4) DRAIN();
        else if (j == 0) { DRAIN(); if (lane == 0) __hip_atomic_store(flag(c, F_KX + 4 * h + (w & 3)), 0u, RLX_AGENT); }
        BAR();
        if (c.tid == 192 && i > 0) __hip_atomic_store(flag(c, F_PT + h), (u32)(i - 1), RLX_AGENT);
    }
    DRAIN();
    BAR();
    if (c.tid == 0) __hip_atomic_store(flag(c, F_PT + h), (u32)(NSEG - 1), RLX_AGENT);
}

DEV void role_vox(const Params& P, const Ctx& c, int h) {
    const int w = c.wave, n = c.n, g = c.g, lane = c.lane;
    u32x8 wf[20];
    if (w < 4) x_stage(c, P.x, 0, c.lds, w);
    load_vo(P, c, h, wf);
    lchar* ximg = c.lds;
    lchar* vimg = c.lds + 81920;
    DRAIN();
    BAR();
    const u32 u_vo = OFF_U + (u32)((10 * (w & 3) + (n >> 3)) * 32 + h) * 1024 + (u32)(g * 16 + (n & 7)) * 16;
    for (int i = 0; i <= NSEG; ++i) {
        if (w < 4) {
            if (i < NSEG) {
                u32x4 o[2];
                tok_proj<true>(c, ximg + (i & 1) * 40960, wf, wf + 10, o, x_dma(c, P.x, i + 1, ximg + ((i + 1) & 1) * 40960, w));
#pragma unroll
                for (int tt = 0; tt < 2; ++tt) *(LAS u32x4*)(vimg + (i & 1) * 8192 + (tt * 4 + w) * 1024 + lane * 16) = o[tt];
            }
        } else if (i > 0) {
            u32x4 a[8];
#pragma unroll
            for (int k = 0; k < 8; ++k) a[k] = lds4(vimg + ((i - 1) & 1) * 8192 + k * 1024 + lane * 16);
            f32x4 d[11][2];
#pragma unroll
            for (int q = 0; q <= 10; ++q) {
                if (q < 10) {
                    d[q][0] = (f32x4){0.f, 0.f, 0.f, 0.f}; d[q][1] = d[q][0];
#pragma unroll
                    for (int ks = 0; ks < 2; ++ks) {
                        const u32x4 b = ks ? hi4(wf[2 * q]) : lo4(wf[2 * q]);
                        d[q][0] = mma16(a[ks], b, d[q][0]);
                        d[q][1] = mma16(a[4 + ks], b, d[q][1]);
                    }
                    SB();
                }
                if (q > 0) {
                    const f32x4 e0 = d[q - 1][0], e1 = d[q - 1][1];
                    st4((u32x4){pk(e0[0], e0[1]), pk(e1[0], e1[1]), pk(e0[2], e0[3]), pk(e1[2], e1[3])}, c.rs,
                        u_vo + ((q - 1) >> 1) * 65536 + ((q - 1) & 1) * 128, (u32)(i - 1) * 1310720u);
                }
                if (q < 10) {
#pragma unroll
                    for (int ks = 2; ks < 4; ++ks) {
                        const u32x4 b = (ks & 1) ? hi4(wf[2 * q + 1]) : lo4(wf[2 * q + 1]);
                        d[q][0] = mma16(a[ks], b, d[q][0]);
                        d[q][1] = mma16(a[4 + ks], b, d[q][1]);
                    }
                    SB();
                }
            }
        }
        const bool full = i <= 3 || i == NSEG;
        if (i > 0) { TSTAMP(0, i - 1, 1); TSTAMP(256, i - 1, 3); }
        if (w < 4 || full) DRAIN(); else asm volatile("s_waitcnt vmcnt(10)" ::: "memory");
        BAR();
        if (c.tid == 0 && i > 0) __hip_atomic_store(flag(c, F_U + h), (u32)(full ? i - 1 : i - 2), RLX_AGENT);
        if (i > 0) { STAMP(224 + h, i - 1, 0); }
    }
}

DEV void wrk_barrier(const Ctx& c, int fi, int arrivals, bool arrive) {
    DRAIN();
    BAR();
    if (c.wave == 0) {
        if (c.lane == 0 && arrive) __hip_atomic_fetch_add(flag(c, fi), 1u, RLX_AGENT);
        wait_ge<16>(c, fi, arrivals - 1);
    }
    BAR();
}

DEV void side_jobs(const Params& P, const Ctx& c, int wid) {
    const int n = c.n, g = c.g;
    const rsrc_t rw = mk_rsrc(P.wo, 640 * 640 * 2), rx = mk_rsrc(P.x, 2048 * 640 * 2);
#pragma unroll 4
    for (int i = 0; i < 11; ++i) {
        const int t = wid + 44 * i, cg = t / 24, ks = t % 24, ga = 4 * ks + g, hd = ga / 3, u = ga % 3;
        u32 d[8];
#pragma unroll
        for (int j = 0; j < 8; ++j) {
            const int dd = u < 2 ? 4 * u + (j & 3) + 8 * (j >> 2) : 16 + j;
            d[j] = pld1(rw, dd < 20 && t < 480 ? (u32)((20 * hd + dd) * 640 + 32 * cg + 2 * n) * 2 : 0x80000000u, 0);
        }
        u32x4 a, b;
#pragma unroll
        for (int j = 0; j < 4; ++j) { a[j] = __builtin_amdgcn_perm(d[2 * j + 1], d[2 * j], 0x05040100); b[j] = __builtin_amdgcn_perm(d[2 * j + 1], d[2 * j], 0x07060302); }
        const u32 o = t < 480 ? OFF_WOF + (u32)((2 * cg + (n >> 3)) * 24 + ks) * 1024 + (u32)(g * 16 + (n & 7)) * 16 : 0x80000000u;
        st4(a, c.rs, o, 0);
        st4(b, c.rs, o + 128, 0);
    }
    u32x4 gv[5];
#pragma unroll
    for (int k = 0; k < 5; ++k) gv[k] = *(const GAS u32x4*)(P.gw + 8 * (n + 16 * k));
#pragma unroll 4
    for (int i = 0; i < 12; ++i) {
        const int t = (wid + 44 * i) * 4 + g;
        float s = 0.f;
#pragma unroll
        for (int k = 0; k < 5; ++k) {
            const u32x4 xv = pld4(rx, (u32)t * 1280 + (u32)(n + 16 * k) * 16, 0);
#pragma unroll
            for (int j = 0; j < 4; ++j) {
                const u32 pr = pk(bf_lo(xv[j]) * bf_lo(gv[k][j]), bf_hi(xv[j]) * bf_hi(gv[k][j]));
                s += bf_lo(pr) + bf_hi(pr);
            }
        }
#pragma unroll
        for (int o = 8; o; o >>= 1) s += __shfl_xor(s, o);
        st1(__float_as_uint(1.0f / (1.0f + __expf(-s))), c.rs, n == 0 && t < 2048 ? OFF_G + (u32)t * 4 : 0x80000000u, 0);
    }
}
DEV void wrk_qkv(const Params& P, const Ctx& c, int wk) {
    const int w = c.wave, n = c.n, g = c.g;
    const int G60 = 8 * (wk & 7) + w, rr = wk >> 3;
    const int s0 = 6 * rr, s1 = min(s0 + 6, NSEG);
    const int kind = G60 / 20, G = G60 % 20;
    u32x8 w0[10], w1[10];
    if (w < 4) x_stage(c, P.x, s0, c.lds + (s0 & 1) * 40960, w);
    int cp = 16 * G + n;
    u32 eo, es;
    if (kind < 2) {
        const int gi = 4 * G + (n >> 2), k = gi % 5;
        cp = 20 * (gi / 5) + (k < 4 ? 10 * (k >> 1) + 4 * (k & 1) + (n & 3) : 10 * ((n >> 1) & 1) + 8 + (n & 1));
        const int go = 4 * G + g, pr = go / 5, ko = go % 5;
        eo = ko < 4 ? OFF_Q0 + (u32)(kind * 32 + 2 * pr + (ko >> 1)) * 65536 + (u32)((ko & 1) * 32 + n) * 16
                    : OFF_Q1 + (u32)(kind * 16 + pr) * 32768 + (u32)n * 16;
        es = ko < 4 ? 1024 : 512;
    } else {
        eo = OFF_VT + (u32)((g & 1) * 2 + (g >> 1)) * 5120 + (u32)cp * 16;
        es = 40960;
    }
    if (kind < 3) load_w<10, 1920>(mk_rsrc(P.wqkv, 640 * 1920 * 2), (u32)kind * 1280, cp, g, w0, w1);
    else side_jobs(P, c, rr * 4 + (w - 4));
    lchar* ximg = c.lds;
    DRAIN();
    BAR();
    const float qs = kind == 0 ? 0.22360679774997896f * LOG2E : 1.0f;
    for (int s = s0; s < s1; ++s) {
        const XDma dm = x_dma(c, P.x, s + 1, ximg + ((s + 1) & 1) * 40960, w & 3);
        f32x4 acc[2][2];
        if (kind < 2) {
            if (w < 4) tok_acc<true, false, false>(c, ximg + (s & 1) * 40960, w0, w1, acc, dm, nullptr, 0);
            else tok_acc<false, false, false>(c, ximg + (s & 1) * 40960, w0, w1, acc, dm, nullptr, 0);
#pragma unroll
            for (int tt = 0; tt < 2; ++tt)
                st4((u32x4){pk(acc[tt][0][0] * qs, acc[tt][1][0] * qs), pk(acc[tt][0][1] * qs, acc[tt][1][1] * qs),
                            pk(acc[tt][0][2] * qs, acc[tt][1][2] * qs), pk(acc[tt][0][3] * qs, acc[tt][1][3] * qs)}, c.rs, eo + (u32)s * es + tt * 256, 0);
        } else if (kind == 2) {
            if (w < 4) tok_acc<true, false, true>(c, ximg + (s & 1) * 40960, w0, w1, acc, dm, nullptr, 0);
            else tok_acc<false, false, true>(c, ximg + (s & 1) * 40960, w0, w1, acc, dm, nullptr, 0);
#pragma unroll
            for (int tt = 0; tt < 2; ++tt) {
                auto r0 = __builtin_amdgcn_permlane32_swap(pk(acc[tt][0][0], acc[tt][0][1]), pk(acc[tt][1][0], acc[tt][1][1]), false, false);
                auto r1 = __builtin_amdgcn_permlane32_swap(pk(acc[tt][0][2], acc[tt][0][3]), pk(acc[tt][1][2], acc[tt][1][3]), false, false);
                st4((u32x4){r0[0], r1[0], r0[1], r1[1]}, c.rs, eo + (u32)s * es + tt * 20480, 0);
            }
        }
        asm volatile("s_waitcnt vmcnt(2)" ::: "memory");
        BAR();
    }
}

constexpr float RESCALE_THR = 6.0f;
DEV float row_max16(const f32x16& s) {
    const float a = fmx(fmx(fmx(fmx(s[0], s[1]), s[2]), fmx(fmx(s[3], s[4]), s[5])), fmx(fmx(s[6], s[7]), s[8]));
    const float b = fmx(fmx(fmx(s[9], s[10]), s[11]), fmx(fmx(s[12], s[13]), s[14]));
    return fmx(fmx(a, b), s[15]);
}
template <int OFF>
DEV void v_read(u32x4& a, u32x4& b, lchar* p) {
    asm volatile("s_mov_b64 exec, %3\n\tds_read_b128 %0, %2 offset:%4\n\tds_read_b128 %1, %2 offset:%5\n\ts_mov_b64 exec, -1"
                 : "+v"(a), "+v"(b) : "v"(p), "s"(0x000FFFFF000FFFFFull), "i"(OFF), "i"(OFF + 640) : "memory");
}
template <int ODD>
DEV void attn_step(lchar* vp, lchar* k0p, lchar* k1p, const u32x4& q0, const u32x4& q1, u32x4& k0, u32x4& k1, const u32x4& v0, const u32x4& v1,
                   u32x4& vn0, u32x4& vn1, const f32x16& cur, f32x16& nxt, f32x16& cm, f32x16& o, bool last = false) {
    float e[16];
    u32 p[8];
    nxt = mma32(k0, q0, cm);
    SB();
    k0 = lds4(k0p + (2 + ODD) * 1024);
#pragma unroll
    for (int i = 0; i < 4; ++i) e[i] = __builtin_amdgcn_exp2f(cur[i]);
    SB();
    nxt = mma32(k1, q1, nxt);
    SB();
    v_read<(1 + ODD) * 1280>(vn0, vn1, vp);
    { const u32x2 t = lds2(k1p + (2 + ODD) * 256); k1.x = t.x; k1.y = t.y; }
#pragma unroll
    for (int i = 4; i < 8; ++i) e[i] = __builtin_amdgcn_exp2f(cur[i]);
#pragma unroll
    for (int i = 0; i < 4; ++i) p[i] = pk(e[2 * i], e[2 * i + 1]);
#pragma unroll
    for (int i = 8; i < 10; ++i) e[i] = __builtin_amdgcn_exp2f(cur[i]);
    SB();
    o = mma32(v0, (u32x4){p[0], p[1], p[2], p[3]}, o);
    SB();
#pragma unroll
    for (int i = 10; i < 16; ++i) e[i] = __builtin_amdgcn_exp2f(cur[i]);
#pragma unroll
    for (int i = 4; i < 8; ++i) p[i] = pk(e[2 * i], e[2 * i + 1]);
    const float m0 = fmx(fmx(nxt[0], nxt[1]), nxt[2]), m1 = fmx(fmx(nxt[3], nxt[4]), nxt[5]);
    SB();
    o = mma32(v1, (u32x4){p[4], p[5], p[6], p[7]}, o);
    SB();
    const float pm = fmx(fmx(fmx(fmx(m0, m1), fmx(fmx(nxt[6], nxt[7]), nxt[8])), fmx(fmx(fmx(nxt[9], nxt[10]), nxt[11]), fmx(fmx(nxt[12], nxt[13]), nxt[14]))), nxt[15]);
    if (__builtin_expect(__builtin_amdgcn_ballot_w64(!(pm <= RESCALE_THR)) != 0 && !last, 0)) {
        const float dl = fmx(max_x32(pm), 0.f);
        const float f = __builtin_amdgcn_exp2f(-dl);
#pragma unroll
        for (int i = 0; i < 16; ++i) { nxt[i] -= dl; cm[i] -= dl; o[i] *= f; }
    }
}
DEV void wrk_attn(const Params& P, const Ctx& c, int wk, gu32* ticket) {
    const int lane = c.lane, tid = c.tid, h = wk >> 1;
    const int q32 = lane & 31, hh = lane >> 5;
#pragma unroll
    for (int r = 0; r < 2; ++r) {
        u32x4 t[4]; u32x2 t1[2]; u32x4 tv[5];
#pragma unroll
        for (int k = 0; k < 4; ++k) t[k] = ld4(c.rs, OFF_Q0 + tid * 16, (u32)(32 + h) * 65536 + (u32)(4 * r + k) * 8192);
#pragma unroll
        for (int k = 0; k < 2; ++k) t1[k] = ld2(c.rs, OFF_Q1 + tid * 16, (u32)(16 + (h >> 1)) * 32768 + (u32)(2 * r + k) * 8192 + (h & 1) * 8);
#pragma unroll
        for (int k = 0; k < 5; ++k) {
            const int v = tid + 512 * (5 * r + k), sh = v / 20, d = v % 20;
            tv[k] = ld4(c.rs, OFF_VT + (u32)(sh * 2 + (d & 1)) * 5120 + (u32)(d >> 1) * 16, (u32)h * 160);
        }
#pragma unroll
        for (int k = 0; k < 4; ++k) *(LAS u32x4*)(c.lds + (4 * r + k) * 8192 + tid * 16) = t[k];
#pragma unroll
        for (int k = 0; k < 2; ++k) *(LAS u32x2*)(c.lds + 65536 + (2 * r + k) * 4096 + tid * 8) = t1[k];
#pragma unroll
        for (int k = 0; k < 5; ++k) *(LAS u32x4*)(c.lds + 81920 + (5 * r + k) * 8192 + tid * 16) = tv[k];
    }
    BAR();
    int task = c.wave;
    while (task < 32) {
        const int qt = (wk & 1) * 32 + task;
        const u32x4 q0 = ld4(c.rs, OFF_Q0 + lane * 16, (u32)(h * 64 + qt) * 1024);
        const u32x2 q1h = ld2(c.rs, OFF_Q1 + q32 * 16, (u32)((h >> 1) * 2048 + qt * 32) * 16 + (h & 1) * 8);
        const u32x4 q1 = {hh ? 0u : q1h.x, hh ? 0u : q1h.y, 0u, 0u};
        lchar* k0p = c.lds + lane * 16;
        lchar* k1p = c.lds + 65536 + q32 * 8;
        lchar* vp = c.lds + 81920 + (hh * 20 + min(q32, 19)) * 16;
        f32x16 o, cm, sa, sb;
#pragma unroll
        for (int i = 0; i < 16; ++i) { o[i] = 0.f; cm[i] = 0.f; }
        u32x4 va0 = {0x3F803F80u, 0x3F803F80u, 0x3F803F80u, 0x3F803F80u}, va1 = va0, vb0 = va0, vb1 = va0;
        v_read<0>(va0, va1, vp);
        u32x4 k0 = lds4(k0p), k1 = {0u, 0u, 0u, 0u};
        { const u32x2 t = lds2(k1p); k1.x = t.x; k1.y = t.y; }
        sa = mma32(k0, q0, cm);
        sa = mma32(k1, q1, sa);
        SB();
        k0 = lds4(k0p + 1024);
        { const u32x2 t = lds2(k1p + 256); k1.x = t.x; k1.y = t.y; }
        {
            const float dl = max_x32(row_max16(sa));
#pragma unroll
            for (int i = 0; i < 16; ++i) { sa[i] -= dl; cm[i] -= dl; }
        }
        for (int t = 0; t < 64; t += 2) {
            PIN("+v"(vp), "+v"(k0p), "+v"(k1p), "+v"(k1));
            attn_step<0>(vp, k0p, k1p, q0, q1, k0, k1, va0, va1, vb0, vb1, sa, sb, cm, o);
            attn_step<1>(vp, k0p, k1p, q0, q1, k0, k1, vb0, vb1, va0, va1, sb, sa, cm, o, t == 62);
            vp += 2560; k0p += 2048; k1p += 512;
        }
        asm volatile("s_waitcnt lgkmcnt(0)" : "+v"(va0), "+v"(va1) :: "memory");
        u32 nx = 0;
        if (lane == 0) nx = __hip_atomic_fetch_add(ticket, 1u, RLX_AGENT);
        auto lr = __builtin_amdgcn_permlane32_swap(__float_as_uint(o[8]), __float_as_uint(o[8]), false, false);
        const float inv = 1.0f / __uint_as_float(lr[1]);
        const u32 ob = OFF_OA + (u32)(qt * 2 + (q32 >> 4)) * 24576 + (u32)(q32 & 15) * 16;
        st4((u32x4){pk(o[0] * inv, o[1] * inv), pk(o[2] * inv, o[3] * inv), pk(o[4] * inv, o[5] * inv), pk(o[6] * inv, o[7] * inv)}, c.rs, ob + (u32)(3 * h + hh) * 256, 0);
        st4((u32x4){pk(o[8] * inv, o[9] * inv), pk(o[10] * inv, o[11] * inv), 0u, 0u}, c.rs, hh ? 0x80000000u : ob + (u32)(3 * h + 2) * 256, 0);
        task = 9 + (int)__builtin_amdgcn_readfirstlane(nx);
    }
}

template <bool PA, bool PB>
DEV void finish_units(const Params& P, const Ctx& c, int u0, int u1, int stride, int me) {
    const int w = c.wave, n = c.n, g = c.g, lane = c.lane;
    int prog = -1, uprog = -1, done = 0;
    for (int u = u0; u < u1; u += stride) {
        const int s = u / 10, cg = u % 10;
        f32x4 res[2][4];
        float gt[2];
#pragma unroll
        for (int tt = 0; tt < 2; ++tt) gt[tt] = __uint_as_float(ld1(c.rs, OFF_G + (u32)(16 * tt + n) * 4, (u32)s * 128));
#pragma unroll
        for (int i = 0; i < 8; ++i) res[i >> 2][i & 3] = ZERO4;
        if (PA) {
#pragma unroll
            for (int k = 0; k < 3; ++k) {
                u32x4 b[2], a[4];
#pragma unroll
                for (int tt = 0; tt < 2; ++tt) b[tt] = ld4(c.rs, OFF_OA + lane * 16, (u32)((s * 2 + tt) * 24 + 3 * w + k) * 1024);
#pragma unroll
                for (int ct = 0; ct < 4; ++ct) a[ct] = ld4(c.rs, OFF_WOF + lane * 16, (u32)((4 * cg + ct) * 24 + 3 * w + k) * 1024);
#pragma unroll
                for (int i = 0; i < 8; ++i) res[i >> 2][i & 3] = mma16(a[i & 3], b[i >> 2], res[i >> 2][i & 3]);
            }
#pragma unroll
            for (int i = 0; i < 8; ++i) res[i >> 2][i & 3] *= gt[i >> 2];
        }
        f32x4 rb = ZERO4;
        if (PB) {
            u32x4 b[5][2], a[5][4];
            if (uprog < s) uprog = wait4<8>(c, F_U + 4 * w, s);
#pragma unroll
            for (int k = 0; k < 4; ++k)
#pragma unroll
                for (int ct = 0; ct < 4; ++ct) a[k][ct] = ld4(c.rs, OFF_U + (u32)((4 * cg + ct) * 32 + 4 * w + k) * 1024 + lane * 16, (u32)s * 1310720u);
            if (prog < s) {
                u32 sp = SPIN_CAP;
                for (;;) {
                    FENCE();
                    const int v = (int)__hip_atomic_load(flag(c, F_PT + 32 * ((lane >> 2) & 1) + 4 * w + (lane & 3)), RLX_AGENT);
                    if (__builtin_amdgcn_ballot_w64(v < s) == 0) { prog = s; break; }
                    __builtin_amdgcn_s_sleep(6);
                    SPIN_TICK(sp);
                }
            }
#pragma unroll
            for (int k = 0; k < 5; ++k)
#pragma unroll
                for (int tt = 0; tt < 2; ++tt)
                    b[k][tt] = ld4(c.rs, OFF_PT + (k < 4 ? (u32)(tt * 32 + 4 * w + k) * 1024 : 65536u + (u32)(tt * 8 + w) * 1024) + lane * 16, (u32)s * 81920u);
#pragma unroll
            for (int ct = 0; ct < 4; ++ct) a[4][ct] = ld4(c.rs, OFF_YL + (u32)((4 * cg + ct) * 8 + w) * 1024 + lane * 16, (u32)s * 327680u);
            f32x4 acc[2][4];
#pragma unroll
            for (int i = 0; i < 8; ++i) acc[i >> 2][i & 3] = ZERO4;
#pragma unroll
            for (int k = 0; k < 5; ++k)
#pragma unroll
                for (int i = 0; i < 8; ++i) acc[i >> 2][i & 3] = mma16(a[k][i & 3], b[k][i >> 2], acc[i >> 2][i & 3]);
#pragma unroll
            for (int i = 0; i < 8; ++i) res[i >> 2][i & 3] += acc[i >> 2][i & 3] * (1.0f - gt[i >> 2]);
        } else {
            wait_ge<8>(c, F_RB + u % NFB, u / NFB);
            rb = __builtin_bit_cast(f32x4, ld4(c.rs, OFF_RB + (u32)w * 1024 + lane * 16, (u32)u * 8192));
        }
        BAR();
#pragma unroll
        for (int i = 0; i < 8; ++i) *(LAS f32x4*)(c.lds + (w * 8 + i) * 1024 + lane * 16) = res[i >> 2][i & 3];
        BAR();
        f32x4 t = *(LAS f32x4*)(c.lds + w * 1024 + lane * 16);
#pragma unroll
        for (int i = 1; i < 8; ++i) t += *(LAS f32x4*)(c.lds + (i * 8 + w) * 1024 + lane * 16);
        if (!PA) {
            st4(__builtin_bit_cast(u32x4, t), c.rs, OFF_RB + (u32)w * 1024 + lane * 16, (u32)u * 8192);
            DRAIN();
            BAR();
            if (c.tid == 0) __hip_atomic_store(flag(c, F_RB + me), (u32)done, RLX_AGENT);
            ++done;
        } else {
            t += rb;
            auto e0 = __builtin_amdgcn_permlane32_swap(__float_as_uint(t[0]), __float_as_uint(t[2]), false, false);
            auto e1 = __builtin_amdgcn_permlane32_swap(__float_as_uint(t[1]), __float_as_uint(t[3]), false, false);
            const u32x2 ov = {pk(__uint_as_float(e0[0]), __uint_as_float(e0[1])), pk(__uint_as_float(e1[0]), __uint_as_float(e1[1]))};
            const int tok = 32 * s + 16 * (w >> 2) + n, col = 64 * cg + 16 * (w & 3) + 8 * (g & 1) + 4 * (g >> 1);
            *(GAS u32x2*)(P.out + (size_t)tok * 640 + col) = ov;
        }
    }
}

__global__ void __launch_bounds__(512, 2) mega_kernel(Params P) {
    extern __shared__ __attribute__((aligned(16))) unsigned char smem[];
    Ctx c;
    c.rs = __builtin_amdgcn_make_buffer_rsrc(P.ws, 0, (int)WS_BYTES, 0x00020000);
    c.fl = (gu32*)(P.ws + OFF_FLAG);
    c.tid = threadIdx.x;
    c.wave = __builtin_amdgcn_readfirstlane(threadIdx.x >> 6);
    c.lane = threadIdx.x & 63;
    c.n = c.lane & 15;
    c.g = c.lane >> 4;
    c.lds = (lchar*)smem;
    const int b = blockIdx.x;
    {
        u64 pc; u32 len, keep;
        asm volatile("s_getpc_b64 %0\n.Lmk_pc:\n\ts_mov_b32 %1, .Lmk_tail-.Lmk_pc" : "=s"(pc), "=s"(len));
        const u32 off = (u32)((b >> 3) * 64 + c.lane) * 64;
        if (c.wave == 7 && off < len)
            asm volatile("s_mov_b32 %0, m0\n\ts_mov_b32 m0, %3\n\ts_nop 0\n\tglobal_load_lds_dword %1, %2\n\ts_mov_b32 m0, %0"
                         : "=&s"(keep) : "v"(off), "s"(pc), "s"(LDS_BYTES - 4096) : "memory");
    }
    const int idx = (b >> 3) * 4 + (b & 3);
    const int rep = (int)(__builtin_amdgcn_s_getreg(20 | (3 << 11)) & 3u);
    const int ro = (b & 4) == 0 ? idx : 128 + idx;
    if (ro < 32) { if (c.wave < 4) role_q(P, c, ro, rep); else role_k(P, c, ro, rep); DUMP(ro); }
    else if (ro < 64) { role_vom(P, c, ro - 32, rep); DUMP(ro); }
    else if (ro < 104) { role_red(P, c, ro - 64, rep); DUMP(ro); }
    else if (ro < 136) { role_kqx(P, c, ro - 104); DUMP(ro + 88); }
    else if (ro < 168) { role_vox(P, c, ro - 136); DUMP(ro + 88); }
    else {
        {
            const int wk = idx - 40;
            WSTAMP(0);
            if (HELP && wk < 80) { const int o = wk % 40; warm_head(P, c, o < 32, o < 32 ? o : o - 8, wk / 40, 2, o >= 32); }
            wrk_qkv(P, c, wk);
            WSTAMP(1);
            wrk_barrier(c, F_BAR1, NWRK, true);
            WSTAMP(2);
            if (wk < NATT) {
                wrk_attn(P, c, wk, flag(c, F_TK + wk));
                WSTAMP(3);
                wrk_barrier(c, F_BAR2, NATT, true);
                WSTAMP(4);
                finish_units<true, false>(P, c, wk, S_SPLIT * 10, NATT, 0);
            } else {
                finish_units<false, true>(P, c, wk - NATT, S_SPLIT * 10, NFB, wk - NATT);
                WSTAMP(3);
                wrk_barrier(c, F_BAR2, NATT, false);
                WSTAMP(4);
            }
            WSTAMP(6);
            finish_units<true, true>(P, c, S_SPLIT * 10 + wk, NSEG * 10, NWRK, 0);
            WSTAMP(5);
        }
    }
}

extern "C" void kernel_launch(void* const* d_in, const int* in_sizes, int n_in,
                              void* d_out, int out_size, void* d_ws, size_t ws_size,
                              hipStream_t stream) {
    (void)in_sizes; (void)n_in; (void)out_size; (void)ws_size;
    Params P;
    P.x = (const u16*)d_in[0];
    P.wqkv = (const u16*)d_in[1];
    P.wo = (const u16*)d_in[2];
    P.mem0 = (const u16*)d_in[4];
    P.Wq = (const u16*)d_in[5];
    P.Wk = (const u16*)d_in[6];
    P.Wv = (const u16*)d_in[7];
    P.Wo = (const u16*)d_in[8];
    P.gw = (const u16*)d_in[10];
    P.out = (u16*)d_out;
    P.ws = (unsigned char*)(((uintptr_t)d_ws + (2u * MB - 1)) & ~(uintptr_t)(2u * MB - 1));
    (void)hipFuncSetAttribute((const void*)mega_kernel, hipFuncAttributeMaxDynamicSharedMemorySize, LDS_BYTES);
    (void)hipMemsetAsync(P.ws, 0xFF, ARM_BYTES, stream);
    mega_kernel<<<256, 512, LDS_BYTES, stream>>>(P);
}
```

```cpp
#include <hip/hip_runtime.h>
#include <hip/hip_bf16.h>
#include <stdint.h>

typedef unsigned short u16;
typedef unsigned int u32;
typedef unsigned long long u64;
typedef __bf16 bf16x2 __attribute__((ext_vector_type(2)));
typedef __bf16 bf16x8 __attribute__((ext_vector_type(8)));
typedef __bf16 bf16x16 __attribute__((ext_vector_type(16)));
typedef float f32x2 __attribute__((ext_vector_type(2)));
typedef float f32x4 __attribute__((ext_vector_type(4)));
typedef float f32x8 __attribute__((ext_vector_type(8)));
typedef float f32x16 __attribute__((ext_vector_type(16)));
typedef u32 u32x2 __attribute__((ext_vector_type(2)));
typedef u32 u32x4 __attribute__((ext_vector_type(4)));
typedef u32 u32x8 __attribute__((ext_vector_type(8)));
#define GAS __attribute__((address_space(1)))
#define LAS __attribute__((address_space(3)))
typedef GAS u32 gu32;
typedef LAS unsigned char lchar;
#define RLX_AGENT __ATOMIC_RELAXED, __HIP_MEMORY_SCOPE_AGENT
#define DEV __device__ __forceinline__

constexpr int NSEG = 64;
constexpr u32 SENT = 0xFFFFFFFFu;
constexpr u32 MB = 1u << 20;
constexpr u32 OFF_FLAG = 3 * MB;
constexpr u32 ARM_BYTES = OFF_FLAG + 800 * 256;
constexpr u32 OFF_EPOCH = OFF_FLAG + 512 * 1024;
constexpr u32 OFF_U = 4 * MB;
constexpr u32 OFF_YL = OFF_U + 80 * MB;
constexpr u32 OFF_QX = OFF_YL + 20 * MB;
constexpr u32 OFF_KX = OFF_QX + 16 * MB;
constexpr u32 OFF_PT = OFF_KX + 16 * MB;
constexpr u32 OFF_Q0 = OFF_PT + 5 * MB;
constexpr u32 OFF_Q1 = OFF_Q0 + 4 * MB;
constexpr u32 OFF_VT = OFF_Q1 + 1 * MB;
constexpr u32 OFF_OA = OFF_VT + 3 * MB;
constexpr u32 OFF_WOF = OFF_OA + 3 * MB;
constexpr u32 OFF_G = OFF_WOF + 1 * MB;
constexpr u32 OFF_RB = OFF_G + 1 * MB;
constexpr u32 OFF_DBG = OFF_RB + 5 * MB;
constexpr u32 WS_BYTES = OFF_DBG + 2 * MB;
constexpr int F_POL = 0, F_FT = 4, F_BAR3 = 1, F_BAR1 = 2, F_BAR2 = 3, F_TW = 136;
constexpr int F_U = 576, F_PT = F_U + 128, F_YL = F_U + 160;
constexpr int F_YR = 40;
constexpr int F_TK = 512;
constexpr int F_RB = 200;
constexpr int F_QX = 256, F_KX = 384;
constexpr int NWRK = 80, NATT = 64, NFB = NWRK - NATT;
#define S_SPLIT_ 40
constexpr int S_SPLIT = S_SPLIT_;
constexpr int LDS_BYTES = 160 * 1024;
#define HELP 1
#define CODEPF 1
#define T_D 133
#define T_RB 56
#define T_DA 12
#define T_YE 16
#define T_DS 30
#define T_NAP 16
#define AT_PRIO 22
#define T_PUB -8
#define PX_LEAD 6
#define RED_ASM 1
#define T_LOOK 44
#define T_WARM 11
#define F_AHEAD 3
#define PX_NOW 1
#define A_GRP 4
#define O_AUX 0
#define VX_NOW 64
#define F_STAG 22
#define T_FINE 1
#define T_UD 28
constexpr u32 SPIN_CAP = 1u << 21;
constexpr float LOG2E = 1.4426950408889634f;

struct Params {
    const u16 *x, *wqkv, *wo, *mem0, *Wq, *Wk, *Wv, *Wo, *gw;
    u16* out;
    unsigned char* ws;
};

typedef __amdgpu_buffer_rsrc_t rsrc_t;
DEV u64 rt() { return __builtin_amdgcn_s_memrealtime(); }
DEV u32 pk(float a, float b) { f32x2 v = {a, b}; return __builtin_bit_cast(u32, __builtin_convertvector(v, bf16x2)); }
DEV u32 guard(u32 v) { return v < 0xFFFFFFFEu ? v : 0xFFFFFFFEu; }
DEV float fmx(float a, float b) { return __builtin_elementwise_maximum(a, b); }
DEV u32 umx3(u32 a, u32 b, u32 c) { return max(max(a, b), c); }
DEV u32 umx4(u32x4 v) { return max(umx3(v.x, v.y, v.z), v.w); }
DEV float bf_lo(u32 v) { return __uint_as_float(v << 16); }
DEV float bf_hi(u32 v) { return __uint_as_float(v & 0xFFFF0000u); }
DEV u32x4 lo4(const u32x8& v) { return __builtin_shufflevector(v, v, 0, 1, 2, 3); }
DEV u32x4 hi4(const u32x8& v) { return __builtin_shufflevector(v, v, 4, 5, 6, 7); }
DEV u32x8 cat8(u32x4 a, u32x4 b) { return __builtin_shufflevector(a, b, 0, 1, 2, 3, 4, 5, 6, 7); }
DEV f32x4 mma16(u32x4 a, u32x4 b, f32x4 c) {
    return __builtin_amdgcn_mfma_f32_16x16x32_bf16(__builtin_bit_cast(bf16x8, a), __builtin_bit_cast(bf16x8, b), c, 0, 0, 0); }
DEV f32x16 mma32(u32x4 a, u32x4 b, f32x16 c) {
    return __builtin_amdgcn_mfma_f32_32x32x16_bf16(__builtin_bit_cast(bf16x8, a), __builtin_bit_cast(bf16x8, b), c, 0, 0, 0); }
DEV f32x4 smma(u32x4 a, const u32x8& b, f32x4 c, int idx) {
    return __builtin_amdgcn_smfmac_f32_16x16x64_bf16(__builtin_bit_cast(bf16x8, a), __builtin_bit_cast(bf16x16, b), c, idx, 0, 0); }
typedef short i16x4 __attribute__((ext_vector_type(4)));
typedef short i16x8 __attribute__((ext_vector_type(8)));
DEV f32x4 smma32(u32x2 a, u32x4 b, f32x4 c, int idx) {
    return __builtin_amdgcn_smfmac_f32_16x16x32_bf16(__builtin_bit_cast(i16x4, a), __builtin_bit_cast(i16x8, b), c, idx, 0, 0); }
DEV float swap_add(float a, float b) {
    auto r = __builtin_amdgcn_permlane32_swap(__float_as_uint(a), __float_as_uint(b), false, false);
    return __uint_as_float(r[0]) + __uint_as_float(r[1]); }
DEV float ror8(float v) { return __uint_as_float(__builtin_amdgcn_update_dpp(0, __float_as_uint(v), 0x128, 0xf, 0xf, false)); }
DEV float max_x16(float v) { auto r = __builtin_amdgcn_permlane16_swap(__float_as_uint(v), __float_as_uint(v), false, false); return fmx(__uint_as_float(r[0]), __uint_as_float(r[1])); }
DEV float max_x32(float v) { auto r = __builtin_amdgcn_permlane32_swap(__float_as_uint(v), __float_as_uint(v), false, false); return fmx(__uint_as_float(r[0]), __uint_as_float(r[1])); }
DEV float add_x16(float v) { auto r = __builtin_amdgcn_permlane16_swap(__float_as_uint(v), __float_as_uint(v), false, false); return __uint_as_float(r[0]) + __uint_as_float(r[1]); }
DEV float add_x32(float v) { auto r = __builtin_amdgcn_permlane32_swap(__float_as_uint(v), __float_as_uint(v), false, false); return __uint_as_float(r[0]) + __uint_as_float(r[1]); }

DEV u32x4 ld4(rsrc_t rs, u32 vo, u32 so) { return __builtin_amdgcn_raw_buffer_load_b128(rs, vo, so, 16); }
DEV u32x2 ld2(rsrc_t rs, u32 vo, u32 so) { return __builtin_amdgcn_raw_buffer_load_b64(rs, vo, so, 16); }
DEV u32 ld1(rsrc_t rs, u32 vo, u32 so) { return __builtin_amdgcn_raw_buffer_load_b32(rs, vo, so, 16); }
DEV void st4(u32x4 v, rsrc_t rs, u32 vo, u32 so) { __builtin_amdgcn_raw_buffer_store_b128(v, rs, vo + so, 0, 16); }
DEV void st2(u32x2 v, rsrc_t rs, u32 vo, u32 so) { __builtin_amdgcn_raw_buffer_store_b64(v, rs, vo, so, 16); }
DEV void st1(u32 v, rsrc_t rs, u32 vo, u32 so) { __builtin_amdgcn_raw_buffer_store_b32(v, rs, vo, so, 16); }
DEV rsrc_t mk_rsrc(const void* p, u32 bytes) { return __builtin_amdgcn_make_buffer_rsrc((void*)p, 0, (int)bytes, 0x00020000); }
DEV u32x4 pld4(rsrc_t rs, u32 vo, u32 so) { return __builtin_amdgcn_raw_buffer_load_b128(rs, vo, so, 0); }
DEV u32x2 pld2(rsrc_t rs, u32 vo, u32 so) { return __builtin_amdgcn_raw_buffer_load_b64(rs, vo, so, 0); }
DEV u32 pld1(rsrc_t rs, u32 vo, u32 so) { return __builtin_amdgcn_raw_buffer_load_b32(rs, vo, so, 0); }
DEV void pst2(u32x2 v, rsrc_t rs, u32 vo, u32 so) { __builtin_amdgcn_raw_buffer_store_b64(v, rs, vo, so, 0); }
DEV u32x4 lds4(lchar* p) { return *(LAS u32x4*)p; }
DEV u32x2 lds2(lchar* p) { return *(LAS u32x2*)p; }
#define BAR() do { asm volatile("s_waitcnt lgkmcnt(0)" ::: "memory"); __builtin_amdgcn_s_barrier(); asm volatile("" ::: "memory"); } while (0)
#define DRAIN() asm volatile("s_waitcnt vmcnt(0)" ::: "memory")
#define FENCE() asm volatile("" ::: "memory")
#define SB() __builtin_amdgcn_sched_barrier(0)
#define PIN(...) asm volatile("" : __VA_ARGS__)

struct Ctx {
    rsrc_t rs;
    u32x4 rq;
    gu32* fl;
    int tid, wave, lane, n, g;
    lchar* lds;
    int b, r8;
    mutable u32 sink;
};
DEV gu32* flag(const Ctx& c, int i) { return c.fl + i * 64; }
#define SPIN_TICK(cnt) asm volatile("s_sub_u32 %0, %0, 1\n\ts_cbranch_scc0 .Lsp%=\n\ts_endpgm\n.Lsp%=:" : "+s"(cnt) :: "scc", "memory")
template <int NAP = 2>
DEV int wait_ge(const Ctx& c, int fi, int want) {
    u32 sp = SPIN_CAP;
    int v = __builtin_amdgcn_readfirstlane(__hip_atomic_load(flag(c, fi), RLX_AGENT));
    while (v < want) {
        __builtin_amdgcn_s_sleep(NAP);
        SPIN_TICK(sp);
        v = __builtin_amdgcn_readfirstlane(__hip_atomic_load(flag(c, fi), RLX_AGENT));
    }
    return v;
}
typedef GAS u64 gu64;
DEV u64 launch_tag(const Params& P) { return __hip_atomic_load((gu64*)(P.ws + OFF_EPOCH), RLX_AGENT) + 1; }
DEV void wait_armed(const Params& P, const Ctx& c) {
    gu64* p = (gu64*)(P.ws + OFF_EPOCH + 256) + min(c.lane, 39);
    const u64 tag = launch_tag(P);
    u32 sp = SPIN_CAP;
    while (__builtin_amdgcn_ballot_w64(__hip_atomic_load(p, RLX_AGENT) != tag) != 0) { __builtin_amdgcn_s_sleep(8); SPIN_TICK(sp); }
    FENCE();
}
DEV int prog4(const Ctx& c, int fi) {
    FENCE();
    int v = (int)__hip_atomic_load(flag(c, fi + (c.lane & 3)), RLX_AGENT);
    v = min(v, __shfl_xor(v, 1)); v = min(v, __shfl_xor(v, 2));
    return __builtin_amdgcn_readfirstlane(v);
}
template <int NAP>
DEV int wait4(const Ctx& c, int fi, int want) {
    u32 sp = SPIN_CAP;
    int v = prog4(c, fi);
    while (v < want) { __builtin_amdgcn_s_sleep(NAP); SPIN_TICK(sp); v = prog4(c, fi); }
    return v;
}
DEV u32 now32() { u64 t; asm volatile("s_memrealtime %0\n\ts_waitcnt lgkmcnt(0)" : "=s"(t) :: "memory"); return (u32)t; }
DEV void wait_until(u32 t) {
    int d = (int)(t - now32());
    if (d <= 0 || d > 1000) return;
    for (int i = 0; i < 200 && d > T_NAP; ++i) { __builtin_amdgcn_s_sleep(2); d = (int)(t - now32()); }
    for (int i = 0; i < 400 && d > 3; ++i) d = (int)(t - now32());
    d = d < 0 ? 0 : d;
    if (d & 2) asm volatile("s_nop 10");
    if (d & 1) asm volatile("s_nop 4");
}
DEV u32 time_word(const Ctx& c, int s) { return __hip_atomic_load(c.fl + (F_TW * 64 + s * 32), RLX_AGENT); }
DEV u32 time_word_s(const Ctx& c, int s) {
    u32 t;
    asm volatile("s_load_dword %0, %1, %2\n\ts_waitcnt lgkmcnt(0)" : "=s"(t) : "s"(c.fl), "s"(__builtin_amdgcn_readfirstlane(F_TW * 256 + s * 128)) : "memory");
    return t;
}
DEV u32 plan_time(u32 tw) { const u32 n = now32(); return n + (u32)((int)(((~tw >> 4) - n) << 4) >> 4); }
DEV u32 plan_level(u32 tw) { return ~tw & 15u; }
DEV u32 mem_time(u32 tw) { tw = __builtin_amdgcn_readfirstlane(tw); return plan_time(tw) + plan_level(tw) + (T_RB + T_DA); }
DEV u32 slot_base(int slot, u32 pol) { return (u32)slot * MB + (((pol >> (slot >> 1)) & 1u) << 13); }

#define TSTAMP(t, s, k) do { } while (0)
#define CSTAMP(s, k) do { } while (0)
#define DUMP(role_id) do { } while (0)
#define WSTAMP(k) do { } while (0)
#define RSTAMP(k) do { } while (0)
#define STAMP(role_id, s, k) TSTAMP(0, s, k)
#define LMAX(s, k, v) do { } while (0)
#define LINIT() do { } while (0)
#define LPT(k, ref) do { } while (0)
#define LPD(id) do { } while (0)
#define LPI do { } while (0)
#define PDECL do { } while (0)
#define PMARK(k) do { } while (0)
#define PDUMP(role_id) do { } while (0)
DEV u64* smem_dbg(const Ctx& c) { return (u64*)(LAS u64*)(c.lds + LDS_BYTES - 2048); }

DEV void split_pair(u32x8& a, u32x8& b) {
#pragma unroll
    for (int j = 0; j < 8; ++j) {
        const u32 e = a[j], o = b[j];
        a[j] = __builtin_amdgcn_perm(o, e, 0x05040100);
        b[j] = __builtin_amdgcn_perm(o, e, 0x07060302);
    }
}
DEV u32 poll_m_regs(const Ctx& c, u32 vo, u32 so, u32x4& v, u32& u) {
    u32 sp = SPIN_CAP;
    do {
        SPIN_TICK(sp);
        v = ld4(c.rs, vo + c.lane * 16, so);
        u = ld1(c.rs, vo + 1024 + c.lane * 4, so);
    } while (__builtin_amdgcn_ballot_w64(umx3(umx3(v.x, v.y, v.z), v.w, u) == SENT) != 0);
    return SPIN_CAP - 1 - sp;
}
DEV u32 poll_m(const Ctx& c, u32 vo, u32 so, lchar* d4, lchar* d1) {
    u32x4 v; u32 u;
    const u32 tries = poll_m_regs(c, vo, so, v, u);
    *(LAS u32x4*)d4 = v;
    *(LAS u32*)d1 = u;
    return tries;
}
DEV u32 fetch_m(const Ctx& c, u32 t, u32 v4, u32 v1, u32 so, lchar* d4, lchar* d1) {
    wait_until(t - 8);
    u32 sp = SPIN_CAP, tq = t - now32() > 1000u ? now32() : t, td;
    u64 tt;
    asm volatile(
        "s_branch .Lmw%=\n"
        ".Lmx%=:\n\ts_sub_u32 %[sp], %[sp], 1\n\ts_cbranch_scc0 .Lmr%=\n\ts_endpgm\n"
        ".Lmw%=:\n\t"
        "s_memrealtime s[96:97]\n\ts_waitcnt lgkmcnt(0)\n\ts_sub_i32 %[td], %[tq], s96\n\ts_cmp_gt_i32 %[td], 3\n\ts_cbranch_scc1 .Lmw%=\n\t"
        "s_max_i32 %[td], %[td], 0\n\ts_bitcmp1_b32 %[td], 1\n\ts_cbranch_scc0 .Lm2%=\n\ts_nop 10\n"
        ".Lm2%=:\n\ts_bitcmp1_b32 %[td], 0\n\ts_cbranch_scc0 .Lmr%=\n\ts_nop 4\n"
        ".Lmr%=:\n\t"
        "buffer_load_dwordx4 v[236:239], %[v4], %[rs], %[so] offen sc1\n\t"
        "buffer_load_dword v240, %[v1], %[rs], %[so] offen sc1\n\t"
        "s_waitcnt vmcnt(1)\n\tds_write_b128 %[d4], v[236:239]\n\tv_max3_u32 v241, v236, v237, v238\n\t"
        "s_waitcnt vmcnt(0)\n\tds_write_b32 %[d1], v240\n\tv_max3_u32 v241, v241, v239, v240\n\t"
        "v_cmp_eq_u32 vcc, -1, v241\n\ts_cbranch_vccnz .Lmx%=\n\t"
        "s_waitcnt lgkmcnt(0)\n\ts_barrier"
        : [sp] "+s"(sp), [td] "=&s"(td), "=&{s[96:97]}"(tt)
        : [v4] "v"(v4), [v1] "v"(v1), [rs] "s"(c.rq), [so] "s"(so), [tq] "s"(tq), [d4] "v"(d4), [d1] "v"(d1)
        : "memory", "scc", "vcc", "v236", "v237", "v238", "v239", "v240", "v241");
    return SPIN_CAP - sp;
}
template <int PF = 5>
DEV void mem_proj(lchar* p, const u32x8* w0, const u32x8* w1, int idx, f32x4 a0, f32x4 a1, u32& oa, u32& ob,
                  lchar* xp = nullptr, u32x4* xf = nullptr) {
    u32x4 a[10];
#pragma unroll
    for (int t = 0; t < PF; ++t) a[t] = lds4(p + t * 1024);
    SB();
#pragma unroll
    for (int t = 0; t < 10; ++t) {
        a0 = smma(a[t], w0[t], a0, idx);
        a1 = smma(a[t], w1[t], a1, idx);
        if (t + PF < 10) a[t + PF] = lds4(p + (t + PF) * 1024);
        if (PF == 10 && t >= 2) xf[t - 2] = lds4(xp + (t - 2) * 1024);
        SB();
    }
    asm("s_nop 7\n\tv_permlane32_swap_b32 v244, v246\n\tv_permlane32_swap_b32 v248, v250\n\tv_permlane32_swap_b32 v245, v247\n\tv_permlane32_swap_b32 v249, v251\n\t"
        "v_add_f32 v244, v244, v246\n\tv_add_f32 v248, v248, v250\n\tv_add_f32 v245, v245, v247\n\tv_add_f32 v249, v249, v251\n\t"
        "v_cvt_pk_bf16_f32 %0, v244, v248\n\tv_cvt_pk_bf16_f32 %1, v245, v249"
        : "=&v"(oa), "=&v"(ob), "+{v[244:247]}"(a0), "+{v[248:251]}"(a1));
}
#define PINZ(a0, a1) asm volatile("" : "+{v[244:247]}"(a0), "+{v[248:251]}"(a1))
#define ZERO4 ((f32x4){0.f, 0.f, 0.f, 0.f})
struct XDma { const u16* src; u32 la, vo; };
DEV XDma x_dma(const Ctx& c, const u16* x, int seg, lchar* dst, int rb) {
    XDma d;
    seg = seg < NSEG ? seg : NSEG - 1;
    d.src = x;
    d.la = __builtin_amdgcn_readfirstlane((u32)(uintptr_t)dst + (u32)rb * 10240);
    d.vo = (u32)((32 * seg + 8 * rb + (c.lane >> 3)) * 1280 + ((c.lane & 7) ^ (c.lane >> 3)) * 16);
    return d;
}
template <int K>
DEV void x_piece(const XDma& d) {
    u32 keep;
    asm volatile("s_mov_b32 %0, m0\n\ts_add_u32 m0, %3, %4\n\ts_nop 0\n\tglobal_load_lds_dwordx4 %1, %2 offset:%5\n\ts_mov_b32 m0, %0"
                 : "=&s"(keep) : "v"(d.vo), "s"(d.src), "s"(d.la), "i"(K * 896), "i"(K * 128) : "memory", "scc");
}
DEV void x_piece_rt(const XDma& d, int k) {
    switch (k) { case 0: x_piece<0>(d); break; case 1: x_piece<1>(d); break; case 2: x_piece<2>(d); break; case 3: x_piece<3>(d); break;
                 case 4: x_piece<4>(d); break; case 5: x_piece<5>(d); break; case 6: x_piece<6>(d); break; case 7: x_piece<7>(d); break;
                 case 8: x_piece<8>(d); break; default: x_piece<9>(d); break; }
}
DEV void x_stage(const Ctx& c, const u16* x, int seg, lchar* dst, int rb) {
    const XDma d = x_dma(c, x, seg, dst, rb);
#pragma unroll
    for (int k = 0; k < 10; ++k) x_piece_rt(d, k);
}
DEV int x_lane_off(const Ctx& c) { return (c.n >> 3) * 10240 + (c.n & 7) * 128 + ((c.g ^ (c.n & 7)) * 16); }
DEV u32x4 x_frag(lchar* pe, lchar* po, int tt, int ks) { return lds4(((ks & 1) ? po : pe) + tt * 20480 + (ks >> 1) * 1024); }
template <bool DMA, bool FLAG, bool SWAP>
DEV void tok_acc(const Ctx& c, lchar* ximg, const u32x8* w0, const u32x8* w1, f32x4 (&acc)[2][2], const XDma& dm, gu32* fl, u32 fv) {
#pragma unroll
    for (int i = 0; i < 4; ++i) acc[i >> 1][i & 1] = (f32x4){0.f, 0.f, 0.f, 0.f};
    const int lo = x_lane_off(c);
    lchar* pe = ximg + lo;
    lchar* po = ximg + (lo ^ 64);
    u32x4 b[20][2];
#pragma unroll
    for (int ks = 0; ks < 3; ++ks) { b[ks][0] = x_frag(pe, po, 0, ks); b[ks][1] = x_frag(pe, po, 1, ks); }
    SB();
#pragma unroll
    for (int ks = 0; ks < 20; ++ks) {
        const u32x4 a0 = (ks & 1) ? hi4(w0[ks >> 1]) : lo4(w0[ks >> 1]);
        const u32x4 a1 = (ks & 1) ? hi4(w1[ks >> 1]) : lo4(w1[ks >> 1]);
        acc[0][0] = SWAP ? mma16(b[ks][0], a0, acc[0][0]) : mma16(a0, b[ks][0], acc[0][0]);
        SB();
        if (ks + 3 < 20) b[ks + 3][0] = x_frag(pe, po, 0, ks + 3);
        SB();
        acc[0][1] = SWAP ? mma16(b[ks][0], a1, acc[0][1]) : mma16(a1, b[ks][0], acc[0][1]);
        SB();
        acc[1][0] = SWAP ? mma16(b[ks][1], a0, acc[1][0]) : mma16(a0, b[ks][1], acc[1][0]);
        SB();
        if (ks + 3 < 20) b[ks + 3][1] = x_frag(pe, po, 1, ks + 3);
        SB();
        acc[1][1] = SWAP ? mma16(b[ks][1], a1, acc[1][1]) : mma16(a1, b[ks][1], acc[1][1]);
        SB();
        if (FLAG && !DMA && ks == 0 && (int)fv < NSEG) {
            DRAIN();
            __hip_atomic_store(fl, fv, RLX_AGENT);
        }
        if (DMA && ks < 10) x_piece_rt(dm, ks);
        SB();
    }
}
template <bool DMA, bool FLAG = false>
DEV void tok_proj(const Ctx& c, lchar* ximg, const u32x8* w0, const u32x8* w1, u32x4 (&o)[2], const XDma& dm, gu32* fl = nullptr, u32 fv = 0) {
    f32x4 acc[2][2];
    tok_acc<DMA, FLAG, false>(c, ximg, w0, w1, acc, dm, fl, fv);
#pragma unroll
    for (int tt = 0; tt < 2; ++tt)
#pragma unroll
        for (int r = 0; r < 4; ++r) o[tt][r] = pk(acc[tt][0][r], acc[tt][1][r]);
}

DEV void check_role(const Params& P, const Ctx& c, u64 tag, u64 ag) {
    gu64* agree = (gu64*)(P.ws + OFF_EPOCH + 128);
    u32 sp = SPIN_CAP;
    while (__builtin_amdgcn_ballot_w64(((ag ^ (tag << 3)) >> 3) != 0) != 0) { SPIN_TICK(sp); ag = __hip_atomic_load(agree, RLX_AGENT); }
    const int r2 = __builtin_amdgcn_readfirstlane((int)ag & 7);
    if (__builtin_expect(r2 != c.r8, 0))
        asm volatile("s_waitcnt vmcnt(0) lgkmcnt(0)\n\ts_barrier\n\ts_getpc_b64 s[4:5]\n.Lre%=:\n\t"
                     "s_sub_u32 s4, s4, .Lre%=-mega_kernel\n\ts_subb_u32 s5, s5, 0\n\ts_setpc_b64 s[4:5]"
                     :: "{s[0:1]}"(__builtin_amdgcn_kernarg_segment_ptr()), "{s2}"(c.b | ((8 + r2) << 16)), "{v0}"(c.tid) : "memory", "s4", "s5", "scc");
}
DEV void check_role(const Params& P, const Ctx& c) { check_role(P, c, launch_tag(P), __hip_atomic_load((gu64*)(P.ws + OFF_EPOCH + 128), RLX_AGENT)); }
template <int LD, int SA = 4>
DEV void fetch_w(const Params& P, const Ctx& c, const u16* W, u32 base, int s1, int s2, u32x8* w0, u32x8* w1) {
    const rsrc_t rw = mk_rsrc(W, LD == 3840 ? 640 * 3840 : 640 * 8192);
    const u32 vo = (u32)((SA == 16 ? 4 : 8) * c.g * LD + c.n * 4);
    u64 tag, ag;
#pragma unroll
    for (int q = 0; q < 10; ++q) {
        if (q == 6) { SB(); tag = launch_tag(P); ag = __hip_atomic_load((gu64*)(P.ws + OFF_EPOCH + 128), RLX_AGENT); SB(); }
#pragma unroll
        for (int j = 0; j < 8; ++j) {
            const u32 o = base + (u32)((q / 5) * s1 + (q % 5) * s2 + (32 * (j >> 2) + SA * ((j >> 1) & 1) + 2 * (j & 1)) * LD);
            if (SA == 16) {
                w0[4 * (q % 5) + 2 * (q / 5) + (j >> 2)][2 * (j & 3)] = pld1(rw, vo, o);
                w0[4 * (q % 5) + 2 * (q / 5) + (j >> 2)][2 * (j & 3) + 1] = pld1(rw, vo, o + LD);
            } else {
                w0[q][j] = pld1(rw, vo, o);
                w1[q][j] = pld1(rw, vo, o + LD);
            }
        }
    }
    SB();
    check_role(P, c, tag, ag);
}
constexpr int KS = 64 * 8192;
template <bool REV = false>
DEV void first_proj(const Ctx& c, lchar* ximg, u32x8* w0, u32x8* w1, u32x4 (&o)[2]) {
    f32x4 acc[2][2] = {{ZERO4, ZERO4}, {ZERO4, ZERO4}};
    const int lo = x_lane_off(c);
#pragma unroll
    for (int kk = 0; kk < 10; ++kk) {
        const int k = REV ? 9 - kk : kk;
        split_pair(w0[k], w1[k]);
#pragma unroll
        for (int i = 0; i < 4; ++i) {
            const u32x4 b = x_frag(ximg + lo, ximg + (lo ^ 64), i & 1, 2 * k + (i >> 1));
            acc[i & 1][0] = mma16((i >> 1) ? hi4(w0[k]) : lo4(w0[k]), b, acc[i & 1][0]);
            acc[i & 1][1] = mma16((i >> 1) ? hi4(w1[k]) : lo4(w1[k]), b, acc[i & 1][1]);
        }
        SB();
    }
#pragma unroll
    for (int tt = 0; tt < 2; ++tt)
#pragma unroll
        for (int r = 0; r < 4; ++r) o[tt][r] = pk(acc[tt][0][r], acc[tt][1][r]);
}
#define STAGED() do { SB(); asm volatile("s_waitcnt vmcnt(60)" ::: "memory"); BAR(); SB(); } while (0)

DEV void warm_rows(const Ctx& c, const u16* W, u32 base, u32 ld, int r0, int r1) {
    const rsrc_t rw = mk_rsrc(W, 640 * 4096 * 2);
    const int ra = r1 - 1 - (c.tid >> 1), rb = ra - 256;
    const u32 a = pld1(rw, ra >= r0 ? (u32)ra * ld + (c.tid & 1) * 128 : 0x80000000u, base);
    const u32 b = pld1(rw, rb >= r0 ? (u32)rb * ld + (c.tid & 1) * 128 : 0x80000000u, base);
    c.sink ^= a ^ b;
}
DEV void warm_head(const Params& P, const Ctx& c, int h, int part, int parts, int rows = 320) {
    const int n = rows / parts, r0 = 640 - rows + part * n;
    warm_rows(c, P.Wv, (u32)h * 256, 8192, r0, r0 + n);
    warm_rows(c, P.Wo, (u32)h * 163840, 256, r0, r0 + n);
}
DEV void warm_q(const Params& P, const Ctx& c, int h) { warm_rows(c, P.Wq, (u32)h * 256, 8192, 320, 640); }

DEV u32 qk_p_vo(const Ctx& c, int h) {
    const int w = c.wave & 3, n = c.n, g = c.g;
    return (g & 1) == 0 ? (u32)((8 + 3 * w + (h >> 4)) * 16384 + ((h >> 1) & 7) * 1024 + ((2 * (h & 1) + (g >> 1)) * 16 + n) * 16)
                        : (c.lane < 24 ? (u32)((8 + 3 * w + 2) * 16384 + h * 128 + n * 16) : 0x80000000u);
}
DEV u32 qk_pt_vo(const Ctx& c, int h) {
    const int w = c.wave & 3, n = c.n, g = c.g, tn = 8 * (w & 1) + (n & 7);
    return n < 8 ? OFF_PT + (u32)((w >> 1) * 32 + h) * 1024 + (u32)(g * 16 + tn) * 16
                 : (g == 0 ? OFF_PT + 65536u + (u32)((w >> 1) * 8 + (h >> 2)) * 1024 + (u32)((h & 3) * 16 + tn) * 16 : 0x80000000u);
}
DEV void role_q(const Params& P, const Ctx& c, int h, int rep) {
    const int w = c.wave, n = c.n, g = c.g, lane = c.lane;
    u32x8 w0[10], w1[10];
    fetch_w<8192>(P, c, P.Wq, (u32)(h * 128 + 32 * w) * 2, 5 * KS, KS, w0, w1);
    STAGED();
#pragma unroll
    for (int k = 0; k < 10; ++k) split_pair(w0[k], w1[k]);
    RSTAMP(1);
    wait_armed(P, c);
    const u32 pol = (u32)wait_ge(c, F_POL, 0);
    RSTAMP(2);
    lchar* qs = c.lds + 10240;
    const int idx = n < 8 ? 0x4444 : 0xEEEE;
    const u32 m_vo = (u32)(2 * rep * 16384 + w * 1280);
    lchar* q_wr = qs + w * 576 + (n >> 2) * 144 + (4 * (g & 1) + 2 * (g >> 1)) * 16 + (n & 3) * 4;
    lchar* q_rd = qs + g * 144 + (n & 7) * 16;
    const int pr = (n & 1) | ((n & 2) << 1) | ((n & 4) >> 1);
    lchar* k_rd = qs + 2304 + g * 144 + pr * 16;
    const u32 p_vo = qk_p_vo(c, h);
    const float cs = 0.08838834764831845f * LOG2E;
    u32 t_m = now32();
    lchar* mrd = c.lds + lane * 16; lchar* md4 = mrd + w * 1280; lchar* md1 = c.lds + w * 1280 + 1024 + lane * 4;
    lchar* t_rd = c.lds + 32768 + (w >> 1) * 4096 + (g * 16 + 8 * (w & 1) + (n & 7)) * 16;
    const int lim = 8 * w + (n & 7) - 16 * (n >> 3) - 4 * g;
    auto tok_softmax = [&](const f32x4& sx, const f32x4& sm) -> u32x4 {
        float v[4], e[4];
        float tm = fmx(fmx(fmx(fmx(sx[0], sx[1]), sx[2]), fmx(fmx(sx[3], sm[0]), sm[1])), fmx(sm[2], sm[3]));
        tm = fmx(tm, ror8(tm)); tm = max_x16(tm); tm = max_x32(tm);
        const float tc = -tm * cs;
#pragma unroll
        for (int r = 0; r < 4; ++r) { v[r] = __builtin_amdgcn_exp2f(__builtin_fmaf(sx[r], cs, tc)); e[r] = __builtin_amdgcn_exp2f(__builtin_fmaf(sm[r], cs, tc)); }
        float sum = __builtin_fmaf((e[0] + e[1]) + (e[2] + e[3]), 0.25f, (v[0] + v[1]) + (v[2] + v[3]));
        sum += ror8(sum); sum = add_x16(sum); sum = add_x32(sum);
        const float ti = __builtin_amdgcn_rcpf(sum);
        const u32 x0 = pk(v[0] * ti, v[1] * ti), x1 = pk(v[2] * ti, v[3] * ti), m0 = pk(e[0] * ti, e[1] * ti), m1 = pk(e[2] * ti, e[3] * ti);
        const u32 y0 = __float_as_uint(ror8(__uint_as_float(x0))), y1 = __float_as_uint(ror8(__uint_as_float(x1)));
        auto n0 = __builtin_amdgcn_permlane16_swap(m0, m0, false, false);
        auto n1 = __builtin_amdgcn_permlane16_swap(m1, m1, false, false);
        const bool lo = n < 8;
        return (u32x4){lo ? x0 : m0, lo ? y0 : m1, lo ? x1 : n0[1], lo ? y1 : n1[1]};
    };
    volatile LAS u32* qcnt = (volatile LAS u32*)(c.lds + 15360);
    lchar* c_wr = c.lds + 15360 + 4 * w; lchar* c_rd = c.lds + 15360 + 4 * (lane & 3);
    if (c.tid < 4) qcnt[c.tid] = 0;
    LINIT();
    int slot = 0;
    __builtin_amdgcn_s_setprio(3);
    PDECL; LPI;
    for (int s = 0; s < NSEG - 1; ++s) {
        const u32 sb = slot_base(slot, pol);
        f32x4 z0 = ZERO4, z1 = ZERO4;
        u32 p_cur = p_vo + sb;
        lchar* kx_rd = mrd + 16384 + (s & 1) * 8192;
        PINZ(z0, z1);
        u32 sv = (u32)(s + 1);
        PIN("+v"(sv), "+v"(kx_rd), "+v"(md4), "+v"(md1), "+v"(mrd));
        u32 sp = SPIN_CAP;
        LMAX(s, 0, now32() - t_m);
        sp -= 1 + fetch_m(c, t_m, m_vo + lane * 16, m_vo + 1024 + lane * 4, sb, md4, md1);
        LPT(1, t_m);
        PMARK(1);
        STAMP(h, s, 0);
        u32 oa, ob;
        u32x4 q[4], km[4], kx[8];
        f32x4 d0, d1, d2;
        mem_proj<10>(mrd, w0, w1, idx, z0, z1, oa, ob, kx_rd, kx);
        PIN("+v"(oa), "+v"(ob)); LPT(2, t_m);
        asm volatile(
            "ds_write2_b32 %[qw], %[oa], %[ob] offset1:4\n\t"
            "ds_write_b32 %[cw], %[sv]\n"
            ".Lqa%=:\n\t"
            "ds_read_b32 v248, %[cr]\n\t"
            "ds_read_b128 %[q0], %[qr]\n\tds_read_b128 %[q1], %[qr] offset:576\n\tds_read_b128 %[q2], %[qr] offset:1152\n\tds_read_b128 %[q3], %[qr] offset:1728\n\t"
            "s_sub_u32 %[sp], %[sp], 1\n\ts_cbranch_scc1 .Lqz%=\n\t"
            "s_waitcnt lgkmcnt(4)\n\tv_cmp_ne_u32 vcc, v248, %[sv]\n\ts_cbranch_vccnz .Lqa%=\n\t"
            "s_barrier\n\t"
            "ds_read_b128 %[m0], %[kr]\n\tds_read_b128 %[m1], %[kr] offset:576\n\tds_read_b128 %[m2], %[kr] offset:1152\n\tds_read_b128 v[248:251], %[kr] offset:1728\n\t"
            "s_waitcnt lgkmcnt(7)\n\tv_mfma_f32_16x16x32_bf16 v[236:239], %[x0], %[q0], 0\n\tv_mfma_f32_16x16x32_bf16 v[240:243], %[x4], %[q0], 0\n\t"
            "s_waitcnt lgkmcnt(6)\n\tv_mfma_f32_16x16x32_bf16 v[236:239], %[x1], %[q1], v[236:239]\n\tv_mfma_f32_16x16x32_bf16 v[240:243], %[x5], %[q1], v[240:243]\n\t"
            "s_waitcnt lgkmcnt(5)\n\tv_mfma_f32_16x16x32_bf16 v[236:239], %[x2], %[q2], v[236:239]\n\tv_mfma_f32_16x16x32_bf16 v[240:243], %[x6], %[q2], v[240:243]\n\t"
            "s_waitcnt lgkmcnt(4)\n\tv_mfma_f32_16x16x32_bf16 v[236:239], %[x3], %[q3], v[236:239]\n\tv_mfma_f32_16x16x32_bf16 v[240:243], %[x7], %[q3], v[240:243]\n\t"
            "s_waitcnt lgkmcnt(3)\n\tv_mfma_f32_16x16x32_bf16 v[244:247], %[m0], %[q0], 0\n\t"
            "s_waitcnt lgkmcnt(2)\n\tv_mfma_f32_16x16x32_bf16 v[244:247], %[m1], %[q1], v[244:247]\n\t"
            "s_waitcnt lgkmcnt(1)\n\tv_mfma_f32_16x16x32_bf16 v[244:247], %[m2], %[q2], v[244:247]\n\t"
            "s_waitcnt lgkmcnt(0)\n\tv_mfma_f32_16x16x32_bf16 v[244:247], v[248:251], %[q3], v[244:247]\n\t"
            "v_cndmask_b32 v236, v240, v236, %[lo]\n\tv_cndmask_b32 v237, v241, v237, %[lo]\n\tv_cndmask_b32 v238, v242, v238, %[lo]\n\tv_cndmask_b32 v239, v243, v239, %[lo]\n\t"
            "v_max3_f32 v248, v236, v237, v238\n\tv_max_f32 v248, v248, v239\n\ts_nop 1\n\t"
            "v_max3_f32 v248, v248, v244, v245\n\tv_max3_f32 v248, v248, v246, v247\n\ts_nop 1\n\t"
            "v_max_f32_dpp v248, v248, v248 row_ror:8 row_mask:0xf bank_mask:0xf\n\t"
            "v_mov_b32 v249, v248\n\ts_nop 1\n\tv_permlane16_swap_b32 v248, v249\n\tv_max_f32 v248, v248, v249\n\t"
            "v_mov_b32 v249, v248\n\ts_nop 1\n\tv_permlane32_swap_b32 v248, v249\n\tv_max_f32 v248, v248, v249\n\t"
            "v_mul_f32 v248, %[cs], v248\n\t"
            "v_fma_f32 v244, v244, %[cs], -v248\n\tv_fma_f32 v245, v245, %[cs], -v248\n\tv_exp_f32 v244, v244\n\tv_exp_f32 v245, v245\n\t"
            "v_fma_f32 v246, v246, %[cs], -v248\n\tv_fma_f32 v247, v247, %[cs], -v248\n\tv_exp_f32 v246, v246\n\tv_exp_f32 v247, v247\n\t"
            "v_fma_f32 v236, v236, %[cs], -v248\n\tv_fma_f32 v237, v237, %[cs], -v248\n\tv_exp_f32 v236, v236\n\tv_exp_f32 v237, v237\n\t"
            "v_add_f32 v249, v244, v245\n\tv_add_f32 v250, v246, v247\n\t"
            "v_fma_f32 v238, v238, %[cs], -v248\n\tv_fma_f32 v239, v239, %[cs], -v248\n\tv_exp_f32 v238, v238\n\tv_exp_f32 v239, v239\n\t"
            "v_add_f32 v249, v249, v250\n\tv_add_f32 v251, v236, v237\n\t"
            "v_fmac_f32 v251, 0.25, v249\n\tv_add_f32 v250, v238, v239\n\t"
            "v_add_f32 v248, v250, v251\n\ts_nop 1\n\t"
            "v_add_f32_dpp v248, v248, v248 row_ror:8 row_mask:0xf bank_mask:0xf\n\t"
            "v_mov_b32 v249, v248\n\ts_nop 1\n\tv_permlane16_swap_b32 v248, v249\n\tv_add_f32 v248, v248, v249\n\t"
            "v_mov_b32 v249, v248\n\ts_nop 1\n\tv_permlane32_swap_b32 v248, v249\n\tv_add_f32 v248, v248, v249\n\t"
            "v_rcp_f32 v248, v248\n\ts_nop 0\n\t"
            "v_mul_f32 v236, v236, v248\n\tv_mul_f32 v237, v237, v248\n\tv_mul_f32 v244, v244, v248\n\tv_mul_f32 v245, v245, v248\n\t"
            "v_cvt_pk_bf16_f32 v240, v236, |v237|\n\tv_cvt_pk_bf16_f32 v242, v244, |v245|\n\t"
            "v_mul_f32 v238, v238, v248\n\tv_mul_f32 v239, v239, v248\n\tv_mul_f32 v246, v246, v248\n\tv_mul_f32 v247, v247, v248\n\t"
            "v_cvt_pk_bf16_f32 v241, v238, |v239|\n\tv_cvt_pk_bf16_f32 v243, v246, |v247|\n\t"
            "v_permlane16_swap_b32 v240, v242\n\ts_nop 0\n\tv_permlane16_swap_b32 v241, v243\n\t"
            "buffer_store_dwordx4 v[240:243], %[pv], %[rs], %[sb] offen sc1\n\t"
            "s_nop 1\n\ts_branch .Lqe%=\n"
            ".Lqz%=:\n\ts_endpgm\n"
            ".Lqe%=:"
            : [sp] "+s"(sp), [q0] "=&v"(q[0]), [q1] "=&v"(q[1]), [q2] "=&v"(q[2]), [q3] "=&v"(q[3]),
              [m0] "=&v"(km[0]), [m1] "=&v"(km[1]), [m2] "=&v"(km[2])
            : [qw] "v"(q_wr), [oa] "v"(oa), [ob] "v"(ob), [cw] "v"(c_wr), [cr] "v"(c_rd), [sv] "v"(sv), [qr] "v"(q_rd), [kr] "v"(k_rd),
              [x0] "v"(kx[0]), [x1] "v"(kx[1]), [x2] "v"(kx[2]), [x3] "v"(kx[3]), [x4] "v"(kx[4]), [x5] "v"(kx[5]), [x6] "v"(kx[6]), [x7] "v"(kx[7]),
              [lo] "s"(0x00FF00FF00FF00FFull), [cs] "s"(cs), [pv] "v"(p_vo), [sb] "s"(sb), [rs] "s"(c.rq)
            : "memory", "scc", "vcc", "v236", "v237", "v238", "v239", "v240", "v241", "v242", "v243", "v244", "v245", "v246", "v247", "v248", "v249", "v250", "v251");
        SB(); FENCE(); SB();
        LPT(4, t_m);
        if (s == 0) BAR();
        STAMP(h, s, 1);
        PMARK(5);
        LMAX(s, 2, now32() - t_m);
        LMAX(s, 3, sp < SPIN_CAP - 3);
        {
#pragma unroll
            for (int ks = 0; ks < 4; ++ks) { q[ks] = lds4(t_rd + (s & 1) * 8192 + ks * 1024); km[ks] = lds4(q_rd + 2304 + ks * 576); }
            SB();
            d0 = ZERO4; d1 = ZERO4; d2 = ZERO4;
#pragma unroll
            for (int ks = 0; ks < 4; ++ks) { d0 = mma16(kx[ks], q[ks], d0); d1 = mma16(kx[4 + ks], q[ks], d1); d2 = mma16(km[ks], q[ks], d2); }
            const float NEG = -1.0e9f / 0.08838834764831845f;
#pragma unroll
            for (int r = 0; r < 4; ++r) { const float t0 = d0[r], t1 = d1[r], ts = n < 8 ? t0 : t1; d0[r] = r <= lim ? ts : NEG; }
            *(LAS u32x4*)(mrd + 49152 + (s & 1) * 4096 + w * 1024) = tok_softmax(d0, d2);
        }
        t_m = mem_time(time_word_s(c, s));
        PMARK(7);
        slot = slot == 2 ? 0 : slot + 1;
    }
    {
        fetch_m(c, t_m, m_vo + lane * 16, m_vo + 1024 + lane * 4, slot_base(slot, pol), md4, md1);
        u32x4 q[4];
        f32x4 d0 = ZERO4, d1 = ZERO4, d2 = ZERO4;
#pragma unroll
        for (int ks = 0; ks < 4; ++ks) {
            q[ks] = lds4(t_rd + 8192 + ks * 1024);
            d0 = mma16(lds4(mrd + 16384 + 8192 + ks * 1024), q[ks], d0);
            d1 = mma16(lds4(mrd + 20480 + 8192 + ks * 1024), q[ks], d1);
        }
        const float NEG = -1.0e9f / 0.08838834764831845f;
#pragma unroll
        for (int r = 0; r < 4; ++r) { const float t0 = d0[r], t1 = d1[r], ts = n < 8 ? t0 : t1; d0[r] = r <= lim ? ts : NEG; }
        PIN("+v"(d0));
        BAR();
#pragma unroll
        for (int ks = 0; ks < 4; ++ks) d2 = mma16(lds4(q_rd + 2304 + ks * 576), q[ks], d2);
        Ctx c2 = c;
        c2.lane = (int)__builtin_amdgcn_mbcnt_hi(~0u, __builtin_amdgcn_mbcnt_lo(~0u, 0u)); PIN("+v"(c2.lane));
        c2.n = c2.lane & 15; c2.g = c2.lane >> 4;
        st4(tok_softmax(d0, d2), c.rs, qk_pt_vo(c2, h) + (u32)(NSEG - 1) * 81920u, 0);
        DRAIN();
        BAR();
    }
    asm volatile(".Lmk_tail:");
    PDUMP(h); LPD(h);
}
DEV void role_k(const Params& P, const Ctx& c, int h, int rep) {
    const int w = c.wave & 3, n = c.n, g = c.g, lane = c.lane;
    x_stage(c, P.x, 0, c.lds + 57344, w);
    u32x8 w0[10], w1[10];
    fetch_w<8192>(P, c, P.Wk, (u32)(h * 128 + 32 * w) * 2, 5 * KS, KS, w0, w1);
    STAGED();
    {
        u32x4 o[2];
        first_proj(c, c.lds + 57344, w0, w1, o);
        *(LAS u32x4*)(c.lds + lane * 16 + 16384 + w * 1024) = o[0];
        *(LAS u32x4*)(c.lds + lane * 16 + 20480 + w * 1024) = o[1];
    }
    RSTAMP(1);
    wait_armed(P, c);
    const u32 pol = (u32)wait_ge(c, F_POL, 0);
    RSTAMP(2);
    lchar* ks_ = c.lds + 10240 + 2304;
    const int idx = n < 8 ? 0x4444 : 0xEEEE;
    lchar* k_wr = ks_ + w * 576 + (n >> 2) * 144 + (4 * (g & 1) + 2 * (g >> 1)) * 16 + (n & 3) * 4;
    const u32 p_vo = qk_p_vo(c, h);
    const u32 qx_vo = OFF_QX + (u32)(h * 8 + 2 * w) * 1024 + lane * 16;
    const u32 pt_vo = qk_pt_vo(c, h);
    lchar* mrd = c.lds + lane * 16;
    gu32* f_pt = flag(c, F_PT + h);
    gu32* f_px = flag(c, ((lane & 4) ? F_KX : F_QX) + 4 * h + (lane & 3));
    const u32 m_vo = (u32)((2 * rep + 1) * 16384 + w * 1280);
    lchar* md4 = mrd + (4 + w) * 1280; lchar* md1 = c.lds + (4 + w) * 1280 + 1024 + lane * 4;
    int slot = 0, prog = -1;
    auto fold = [&](int v) {
        v = min(v, __shfl_xor(v, 1)); v = min(v, __shfl_xor(v, 2)); v = min(v, __shfl_xor(v, 4));
        return __builtin_amdgcn_readfirstlane(v);
    };
    u32 t_m = now32();
    PDECL; LPI;
    for (int s = 0; s < NSEG; ++s) {
        const u32 sb = slot_base(slot, pol);
        f32x4 z0 = ZERO4, z1 = ZERO4;
        PINZ(z0, z1);
        const int sn = min(s + 1, NSEG - 1);
        const bool get = prog >= sn;
        const u32 qn = (u32)sn * 262144u, kn = qn + 16 * MB;
        u32 gv = get ? qx_vo : 0x80000000u;
        PIN("+v"(mrd), "+v"(md4), "+v"(md1), "+v"(gv));
        LMAX(s, 1, now32() - t_m);
        fetch_m(c, t_m, m_vo + lane * 16, m_vo + 1024 + lane * 4, sb, md4, md1);
        PMARK(1);
        u32x4 q0, q1, k0, k1;
        u32 oa, ob;
        const u32 tw = time_word(c, s);
        q0 = ld4(c.rs, gv, qn); q1 = ld4(c.rs, gv + 1024, qn); k0 = ld4(c.rs, gv, kn); k1 = ld4(c.rs, gv + 1024, kn);
        SB();
        mem_proj<8>(mrd, w0, w1, idx, z0, z1, oa, ob);
        *(LAS u32*)(k_wr) = oa;
        *(LAS u32*)(k_wr + 16) = ob;
        LPT(2, t_m);
        PMARK(2);
        BAR();
        LPT(3, t_m);
        if (s == 0) {
            wait4<2>(c, F_QX + 4 * h, 0);
            *(LAS u32x4*)(mrd + 32768 + w * 2048) = ld4(c.rs, qx_vo, 0);
            *(LAS u32x4*)(mrd + 32768 + w * 2048 + 1024) = ld4(c.rs, qx_vo + 1024, 0);
            BAR();
        }
        if (w == 3) __hip_atomic_store(f_pt, (u32)max(s - 2, -1), RLX_AGENT);
        st4((u32x4){SENT, SENT, SENT, SENT}, c.rs, p_vo, slot_base(slot == 0 ? 2 : slot - 1, pol));
        st4(lds4(mrd + 49152 + ((s + 1) & 1) * 4096 + w * 1024), c.rs, s > 0 ? pt_vo + (u32)(s - 1) * 81920u : 0x80000000u, 0);
        if (s == NSEG - 1) break;
        int pv = NSEG;
        if (prog <= s + 2) pv = (int)__hip_atomic_load(f_px, RLX_AGENT);
        if (!get) {
            u32 sp = SPIN_CAP;
            while ((prog = fold((int)__hip_atomic_load(f_px, RLX_AGENT))) < sn) { __builtin_amdgcn_s_sleep(2); SPIN_TICK(sp); }
            q0 = ld4(c.rs, qx_vo, qn); q1 = ld4(c.rs, qx_vo + 1024, qn); k0 = ld4(c.rs, qx_vo, kn); k1 = ld4(c.rs, qx_vo + 1024, kn);
            pv = prog;
        }
        *(LAS u32x4*)(mrd + 32768 + ((s + 1) & 1) * 8192 + w * 2048) = q0;
        *(LAS u32x4*)(mrd + 32768 + ((s + 1) & 1) * 8192 + w * 2048 + 1024) = q1;
        *(LAS u32x4*)(mrd + 16384 + ((s + 1) & 1) * 8192 + w * 2048) = k0;
        *(LAS u32x4*)(mrd + 16384 + ((s + 1) & 1) * 8192 + w * 2048 + 1024) = k1;
        if (prog <= s + 2) prog = fold(pv);
        t_m = mem_time(tw);
        PMARK(3);
        slot = slot == 2 ? 0 : slot + 1;
    }
    DRAIN();
    BAR();
    if (w == 3) __hip_atomic_store(f_pt, (u32)(NSEG - 1), RLX_AGENT);
    PDUMP(h); LPD(h);
}

DEV void role_vom(const Params& P, const Ctx& c, int h, int rep) {
    const int w = c.wave, n = c.n, g = c.g, lane = c.lane;
    u32x8 wf[20];
    if (w < 4) {
        fetch_w<8192>(P, c, P.Wv, (u32)(h * 128 + 32 * w) * 2, 5 * KS, KS, wf, wf + 10);
#pragma unroll
        for (int k = 0; k < 10; ++k) split_pair(wf[k], wf[10 + k]);
    } else {
        fetch_w<1280, 16>(P, c, P.Wo, (u32)(h * 128 * 640 + 160 * (w - 4)) * 2, 64 * 1280, 64, wf, nullptr);
#pragma unroll
        for (int q = 0; q < 20; ++q)
#pragma unroll
            for (int u = 0; u < 4; ++u) {
                const u32 e = wf[q][2 * u], o = wf[q][2 * u + 1];
                wf[q][2 * u] = __builtin_amdgcn_perm(o, e, 0x05040100);
                wf[q][2 * u + 1] = __builtin_amdgcn_perm(o, e, 0x07060302);
            }
    }
    RSTAMP(1);
    wait_armed(P, c);
    const u32 pol = (u32)wait_ge(c, F_POL, 0);
    RSTAMP(2);
    lchar* mimg = c.lds;
    lchar* vs = c.lds + 10240;
    const int idx = n < 8 ? 0x4444 : 0xEEEE;
    const u32 m_vo = (u32)((2 * rep + (w >> 2)) * 16384 + (w & 3) * 1280);
    lchar* v_wr = vs + (w & 3) * 576 + (n >> 2) * 144 + (4 * (g & 1) + 2 * (g >> 1)) * 16 + (n & 3) * 4;
    lchar* v_rd = vs + g * 144 + (n & 7) * 16;
    lchar* md4 = mimg + w * 1280 + lane * 16; lchar* md1 = mimg + w * 1280 + 1024 + lane * 4; lchar* mrd = mimg + lane * 16;
    const int r0 = 10 * (w & 3) + (n >> 3);
    const u32 inner = (u32)(h * 256 + (8 * (g >> 1) + (n & 7)) * 16 + (g & 1) * 8);
    const u32 y_vo = (u32)(20 + r0) * 16384 + inner;
    const u32 yl_vo = OFF_YL + (u32)(NSEG - 1) * 327680u + (u32)r0 * 8192 + inner;
    int slot = 0;
    u32 t_m = now32();
    LINIT();
    PDECL; LPI;
    if (w < 4) {
        for (int s = 0; s < NSEG; ++s) {
            const u32 sb = slot_base(slot, pol);
            f32x4 z0 = ZERO4, z1 = ZERO4;
            PINZ(z0, z1);
        PIN("+v"(md4), "+v"(md1), "+v"(mrd));
            fetch_m(c, t_m, m_vo + lane * 16, m_vo + 1024 + lane * 4, sb, md4, md1);
            PMARK(1);
            STAMP(32 + h, s, 0);
            u32 oa, ob;
            mem_proj<9>(mrd, wf, wf + 10, idx, z0, z1, oa, ob);
            *(LAS u32*)(v_wr) = oa;
            *(LAS u32*)(v_wr + 16) = ob;
            PMARK(2);
            BAR();
            t_m = mem_time(time_word_s(c, s));
            slot = slot == 2 ? 0 : slot + 1;
        }
    } else {
        for (int s = 0; s < NSEG; ++s) {
            const u32 sb = slot_base(slot, pol);
            f32x16 da = {}; f32x4 db = ZERO4;
            PIN("+v"(md4), "+v"(md1), "+{v[216:231]}"(da), "+{v[232:235]}"(db));
            fetch_m(c, t_m, m_vo + lane * 16, m_vo + 1024 + lane * 4, sb, md4, md1);
            PMARK(1);
            const bool last = s == NSEG - 1;
            const u32 yv = last ? yl_vo : y_vo, ystep = last ? 16384u : 32768u;
            u32x8 y8; u32x2 y2; u32 so = last ? 0u : sb; f32x16 af;
            asm volatile(
                "s_waitcnt lgkmcnt(0)\n\ts_barrier\n\t"
                "ds_read_b128 v[236:239], %[vr]\n\tds_read_b128 v[240:243], %[vr] offset:576\n\tds_read_b128 v[244:247], %[vr] offset:1152\n\tds_read_b128 v[248:251], %[vr] offset:1728\n\t"
                "s_waitcnt lgkmcnt(3)\n\tv_smfmac_f32_16x16x64_bf16 v[216:219], v[236:239], %[w0], %[ix]\n\t"
                "s_waitcnt lgkmcnt(2)\n\tv_smfmac_f32_16x16x64_bf16 v[216:219], v[240:243], %[w1], %[ix]\n\t"
                "s_waitcnt lgkmcnt(1)\n\tv_smfmac_f32_16x16x64_bf16 v[216:219], v[244:247], %[w2], %[ix]\n\t"
                "s_waitcnt lgkmcnt(0)\n\tv_smfmac_f32_16x16x64_bf16 v[216:219], v[248:251], %[w3], %[ix]\n\t"
                "v_smfmac_f32_16x16x64_bf16 v[220:223], v[236:239], %[w4], %[ix]\n\t"
                "v_smfmac_f32_16x16x64_bf16 v[220:223], v[240:243], %[w5], %[ix]\n\t"
                "v_smfmac_f32_16x16x64_bf16 v[220:223], v[244:247], %[w6], %[ix]\n\t"
                "v_smfmac_f32_16x16x64_bf16 v[220:223], v[248:251], %[w7], %[ix]\n\t"
                "v_smfmac_f32_16x16x64_bf16 v[224:227], v[236:239], %[w8], %[ix]\n\t"
                "v_smfmac_f32_16x16x64_bf16 v[224:227], v[240:243], %[w9], %[ix]\n\t"
                : "+{v[216:231]}"(da), "=&{v[236:251]}"(af)
                : [w0] "v"(wf[0]), [w1] "v"(wf[1]), [w2] "v"(wf[2]), [w3] "v"(wf[3]), [w4] "v"(wf[4]), [w5] "v"(wf[5]), [w6] "v"(wf[6]), [w7] "v"(wf[7]), [w8] "v"(wf[8]), [w9] "v"(wf[9]), [ix] "v"(idx), [vr] "v"(v_rd) : "memory");
            asm volatile(
                "s_nop 1\n\tv_cvt_pk_bf16_f32 v204, v216, v217\n\t"
                "v_smfmac_f32_16x16x64_bf16 v[224:227], v[244:247], %[w10], %[ix]\n\tv_cvt_pk_bf16_f32 v205, v218, v219\n\t"
                "v_smfmac_f32_16x16x64_bf16 v[224:227], v[248:251], %[w11], %[ix]\n\tv_min_u32 v204, -2, v204\n\t"
                "v_smfmac_f32_16x16x64_bf16 v[228:231], v[236:239], %[w12], %[ix]\n\tv_min_u32 v205, -2, v205\n\tbuffer_store_dwordx2 v[204:205], %[yv], %[rs], %[so] offen sc1\n\ts_add_u32 %[so], %[so], %[st]\n\t"
                "v_smfmac_f32_16x16x64_bf16 v[228:231], v[240:243], %[w13], %[ix]\n\tv_cvt_pk_bf16_f32 v206, v220, v221\n\t"
                "v_smfmac_f32_16x16x64_bf16 v[228:231], v[244:247], %[w14], %[ix]\n\tv_cvt_pk_bf16_f32 v207, v222, v223\n\t"
                "v_smfmac_f32_16x16x64_bf16 v[228:231], v[248:251], %[w15], %[ix]\n\tv_min_u32 v206, -2, v206\n\t"
                "v_smfmac_f32_16x16x64_bf16 v[232:235], v[236:239], %[w16], %[ix]\n\tv_min_u32 v207, -2, v207\n\tbuffer_store_dwordx2 v[206:207], %[yv], %[rs], %[so] offen sc1\n\ts_add_u32 %[so], %[so], %[st]\n\t"
                "v_smfmac_f32_16x16x64_bf16 v[232:235], v[240:243], %[w17], %[ix]\n\tv_cvt_pk_bf16_f32 v208, v224, v225\n\t"
                "v_smfmac_f32_16x16x64_bf16 v[232:235], v[244:247], %[w18], %[ix]\n\tv_cvt_pk_bf16_f32 v209, v226, v227\n\t"
                "v_smfmac_f32_16x16x64_bf16 v[232:235], v[248:251], %[w19], %[ix]\n\tv_min_u32 v208, -2, v208\n\t"
                "v_min_u32 v209, -2, v209\n\tbuffer_store_dwordx2 v[208:209], %[yv], %[rs], %[so] offen sc1\n\ts_add_u32 %[so], %[so], %[st]\n\tv_cvt_pk_bf16_f32 v210, v228, v229\n\tv_cvt_pk_bf16_f32 v211, v230, v231\n\tv_min_u32 v210, -2, v210\n\tv_min_u32 v211, -2, v211\n\tbuffer_store_dwordx2 v[210:211], %[yv], %[rs], %[so] offen sc1\n\ts_add_u32 %[so], %[so], %[st]\n\tv_cvt_pk_bf16_f32 v212, v232, v233\n\tv_cvt_pk_bf16_f32 v213, v234, v235\n\tv_min_u32 v212, -2, v212\n\tv_min_u32 v213, -2, v213\n\tbuffer_store_dwordx2 v[212:213], %[yv], %[rs], %[so] offen sc1\n\ts_add_u32 %[so], %[so], %[st]\n\ts_nop 0"
                : "+{v[216:231]}"(da), "+{v[232:235]}"(db), "=&{v[204:211]}"(y8), "=&{v[212:213]}"(y2), [so] "+s"(so)
                : [w10] "v"(wf[10]), [w11] "v"(wf[11]), [w12] "v"(wf[12]), [w13] "v"(wf[13]), [w14] "v"(wf[14]), [w15] "v"(wf[15]), [w16] "v"(wf[16]), [w17] "v"(wf[17]), [w18] "v"(wf[18]), [w19] "v"(wf[19]),
                  "{v[236:251]}"(af), [ix] "v"(idx), [yv] "v"(yv), [st] "s"(ystep), [rs] "s"(c.rq) : "memory", "scc");
            LPT(4, t_m);
            TSTAMP(256, s, 2);
            LMAX(s, 2, now32() - t_m);
            PMARK(4);
            t_m = mem_time(time_word_s(c, s));
            slot = slot == 2 ? 0 : slot + 1;
        }
    }
    DRAIN();
    BAR();
    if (c.tid == 0) __hip_atomic_store(flag(c, F_YL + h), (u32)(NSEG - 1), RLX_AGENT);
    PDUMP(32 + h); LPD(32 + h);
}

DEV u32 probe(const Ctx& c, u32 off) {
    u64 a, b; u32 v;
    asm volatile("s_memtime %0\n\ts_waitcnt lgkmcnt(0)" : "=s"(a) :: "memory");
    v = ld1(c.rs, off, 0);
    asm volatile("s_waitcnt vmcnt(0)" : "+v"(v) :: "memory");
    asm volatile("s_memtime %0\n\ts_waitcnt lgkmcnt(0)" : "=s"(b) :: "memory");
    return (u32)(b - a);
}
DEV void role_red(const Params& P, const Ctx& c, int r, int rep) {
    const int w = c.wave, n = c.n, g = c.g, lane = c.lane;
    const u64 tag = launch_tag(P);
    check_role(P, c);
#pragma unroll
    for (int k = 0; k < 11; ++k) {
        const u32 o = (u32)(k * 40 + r) * 8192 + c.tid * 16;
        st4((u32x4){SENT, SENT, SENT, SENT}, c.rs, o < ARM_BYTES ? o : 0x80000000u, 0);
    }
    DRAIN();
    BAR();
    if (c.tid == 0) __hip_atomic_store((gu64*)(P.ws + OFF_EPOCH + 256) + r, tag, RLX_AGENT);
    if (HELP && r > 0) { if (r < 32) warm_head(P, c, r, 0, 1); else for (int i = 0; i < 2; ++i) warm_q(P, c, 16 + (r & 7) + 8 * i); }
    asm volatile("" :: "v"(c.sink));
    if (r == 0) {
        wait_armed(P, c);
        u32 best[4] = {~0u, ~0u, ~0u, ~0u};
        for (int i = 0; i < 3; ++i) {
#pragma unroll
            for (int k = 0; k < 4; ++k)
                best[k] = min(best[k], probe(c, (u32)(k >> 1) * 2 * MB + 62 * 16384 + (k & 1) * 8192 + (u32)(8 * w + i) * 128));
        }
        LAS u32* bs = (LAS u32*)(c.lds + 16384);
        if (lane == 0) *(LAS u32x4*)(bs + 4 * w) = (u32x4){best[0], best[1], best[2], best[3]};
        BAR();
        if (c.tid == 0) {
            for (int i = 1; i < 8; ++i)
                for (int k = 0; k < 4; ++k) best[k] = min(best[k], bs[4 * i + k]);
            __hip_atomic_store(flag(c, F_POL), (best[1] < best[0] ? 1u : 0u) | (best[3] < best[2] ? 2u : 0u), RLX_AGENT);
        }
    }
    RSTAMP(1);
    wait_armed(P, c);
    const u32 pol = (u32)wait_ge(c, F_POL, 0);
    RSTAMP(2);
    const int idx = n < 8 ? 0x4444 : 0xEEEE;
    const int oi = (lane >> 2) & 7, oq = 2 * (lane & 3) + (lane >> 5);
    const u32 mo = (u32)(r * 256 + lane * 4);
    const u32 m_vo = (u32)((2 * (w & 3) + (r >= 20)) * 16384) + mo - (r >= 20 ? 5120u : 0u);
    if (w < 4) {
        const u32 v = *(const GAS u32*)(P.mem0 + oi * 640 + 16 * r + 2 * oq);
        st1(guard(v), c.rs, m_vo, slot_base(0, pol));
    }
    lchar* r_wr = c.lds + ((4 * (g & 1) + 2 * (g >> 1) + 8 * (n & 1)) * 4 + ((n & 7) >> 1)) * 80 + w * 8 + (n >> 3) * 4;
    lchar* r_rd = c.lds + lane * 80;
    const u32 y_vo = (u32)((20 + r) * 16384 + w * 1024 + lane * 16);
    const u32 rot = (u32)(r >> 2) & 1u;
    const u32 px_vo = (u32)((8 + 3 * rep + (w >> 2)) * 16384 + (w & 3) * 2048 + lane * 16) + rot * 1024, px_vo1 = px_vo ^ 1024u;
    const u32 pm_vo = (u32)((8 + 3 * rep + 2) * 16384 + (4 * w + g) * 128 + (n & 7) * 16 + (n >> 3) * 8);
    const u32 u_vo = OFF_U + (u32)(r * 32 + 4 * w) * 1024 + lane * 16 + rot * 2048, u_vo1 = u_vo ^ 2048u;
    int uprog = -1;
    PDECL; LPI;
    int slot = 0;
    u32x4 y = {0u, 0u, 0u, 0u};
    const u32 yl_vo = OFF_YL + (u32)(r * 8 + w) * 1024 + lane * 16;
    u32 t_p = now32() - 2000u, lv = 0, t_nom = 0, hist = 0;
    LINIT();
    BAR();
    for (int s = 0; s < NSEG - 1; ++s) {
        u32 sp = SPIN_CAP;
        const u32 sb = slot_base(slot, pol);
        const int slotn = slot == 2 ? 0 : slot + 1;
        const u32 t_u = now32() + T_UD;
        while (uprog < s) { SPIN_TICK(sp); uprog = prog4(c, F_U + 32 * (r / 10) + 4 * w); }
        wait_until(t_u);
        if (s > 0) {
            const u32 sbp = slot_base(slot == 0 ? 2 : slot - 1, pol);
            if (w < 4) st1(SENT, c.rs, m_vo, sbp);
            st4(y, c.rs, yl_vo, (u32)(s - 1) * 327680u);
            st4((u32x4){SENT, SENT, SENT, SENT}, c.rs, y_vo, sbp);
            if (w == 6) __hip_atomic_store(flag(c, F_YR + r), (u32)(s - 2), RLX_AGENT);
        }
        const u32 ub = (u32)s * 1310720u;
        u32x8 u0, u1;
        asm volatile("buffer_load_dwordx4 v[140:143], %2, %4, %5 offen sc1\n\tbuffer_load_dwordx4 v[144:147], %2, %4, %5 offen offset:1024 sc1\n\t"
                     "buffer_load_dwordx4 v[148:151], %3, %4, %5 offen sc1\n\tbuffer_load_dwordx4 v[152:155], %3, %4, %5 offen offset:1024 sc1"
                     : "={v[140:147]}"(u0), "={v[148:155]}"(u1) : "v"(u_vo), "v"(u_vo1), "s"(c.rq), "s"(ub) : "memory");
        const u32 m_so = slot_base(slotn, pol);
        const int par = (s & 1) * 5120;
        u32x4 p0, p1; u32x2 ma; (void)p0; (void)p1; (void)ma;
        f32x4 d = ZERO4;
        lchar* rw_ = r_wr + par; lchar* rr_ = r_rd + par;
        PIN("+v"(d), "+v"(rw_), "+v"(rr_));
        wait_until(t_u + (T_D - T_UD - T_YE - T_LOOK));
        {
            const u32 t2 = time_word_s(c, s);
            t_p = plan_time(t2);
            lv = plan_level(t2);
        }
        LMAX(s, 0, now32() - (t_p - T_YE));
        const u32 sp0 = sp; (void)sp0;
        wait_until(t_p - T_YE);
        {
            u32 tq = t_p - now32() > 1000u ? now32() : t_p, td;
            u64 tt;
            asm volatile(
                "buffer_load_dwordx4 v[112:115], %[yv], %[rs], %[sb] offen sc1\n\t"
                "s_branch .Lrw%=\n"
                ".Lrx%=:\n\t"
                "s_sub_u32 %[sp], %[sp], 1\n\ts_cbranch_scc0 .Lry%=\n\ts_endpgm\n"
                ".Lry%=:\n\t"
                "s_nop 7\n\tbuffer_load_dwordx4 v[112:115], %[yv], %[rs], %[sb] offen sc1\n\ts_branch .Lrr%=\n"
                ".Lrw%=:\n\t"
                "s_memrealtime s[96:97]\n\ts_waitcnt lgkmcnt(0)\n\ts_sub_i32 %[td], %[tq], s96\n\ts_cmp_gt_i32 %[td], 3\n\ts_cbranch_scc1 .Lrw%=\n\t"
                "s_max_i32 %[td], %[td], 0\n\ts_bitcmp1_b32 %[td], 1\n\ts_cbranch_scc0 .Lr2%=\n\ts_nop 10\n"
                ".Lr2%=:\n\ts_bitcmp1_b32 %[td], 0\n\ts_cbranch_scc0 .Lrr%=\n\ts_nop 4\n"
                ".Lrr%=:\n\t"
                "buffer_load_dwordx2 v[108:109], %[pm], %[rs], %[sb] offen sc1\n\t"
                "buffer_load_dwordx4 v[100:103], %[p0], %[rs], %[sb] offen sc1\n\t"
                "buffer_load_dwordx4 v[104:107], %[p1], %[rs], %[sb] offen sc1\n\t"
                "v_mov_b32 v116, 0\n\tv_mov_b32 v117, 0\n\tv_mov_b32 v118, 0\n\tv_mov_b32 v119, 0\n\t"
                "s_cmp_gt_u32 %[w], 3\n\t"
                "s_waitcnt vmcnt(3)\n\tv_max_u32 v110, v112, v113\n\tv_max3_u32 v110, v110, v114, v115\n\t"
                "s_waitcnt vmcnt(2)\n\tv_smfmac_f32_16x16x32_bf16 v[116:119], v[108:109], v[112:115], %[ix]\n\t"
                "v_max3_u32 v110, v110, v108, v109\n\ts_nop 4\n\t"
                "s_waitcnt vmcnt(1)\n\tv_smfmac_f32_16x16x64_bf16 v[116:119], v[100:103], v[140:147], %[ix]\n\t"
                "v_max3_u32 v110, v110, v100, v101\n\tv_max3_u32 v110, v110, v102, v103\n\t"
                "s_waitcnt vmcnt(0)\n\tv_smfmac_f32_16x16x64_bf16 v[116:119], v[104:107], v[148:155], %[ix]\n\t"
                "v_max3_u32 v110, v110, v104, v105\n\tv_max3_u32 v110, v110, v106, v107\n\t"
                "v_cmp_eq_u32 vcc, -1, v110\n\ts_cbranch_vccnz .Lrx%=\n\t"
                "s_nop 3\n\t"
                "v_permlane32_swap_b32 v116, v118\n\tv_permlane32_swap_b32 v117, v119\n\t"
                "v_add_f32 v116, v116, v118\n\tv_add_f32 v117, v117, v119\n\t"
                "ds_write2_b32 %[lw], v116, v117 offset1:80\n\t"
                "s_waitcnt lgkmcnt(0)\n\ts_barrier\n\t"
                "s_cbranch_scc1 .Lre%=\n\t"
                "ds_read_b128 v[120:123], %[lr]\n\tds_read_b128 v[124:127], %[lr] offset:16\n\t"
                "ds_read_b128 v[128:131], %[lr] offset:32\n\tds_read_b128 v[132:135], %[lr] offset:48\n\t"
                "s_waitcnt lgkmcnt(3)\n\tv_add_f32 v120, v120, v122\n\tv_add_f32 v121, v121, v123\n\t"
                "s_waitcnt lgkmcnt(2)\n\tv_add_f32 v124, v124, v126\n\tv_add_f32 v125, v125, v127\n\tv_add_f32 v120, v120, v124\n\tv_add_f32 v121, v121, v125\n\t"
                "s_waitcnt lgkmcnt(1)\n\tv_add_f32 v128, v128, v130\n\tv_add_f32 v129, v129, v131\n\tv_add_f32 v120, v120, v128\n\tv_add_f32 v121, v121, v129\n\t"
                "s_waitcnt lgkmcnt(0)\n\tv_add_f32 v132, v132, v134\n\tv_add_f32 v133, v133, v135\n\tv_add_f32 v120, v120, v132\n\tv_add_f32 v121, v121, v133\n\t"
                "v_cvt_pk_bf16_f32 v120, v120, v121\n\tv_min_u32 v120, -2, v120\n\t"
                "buffer_store_dword v120, %[mv], %[rs], %[ms] offen sc1\n"
                ".Lre%=:"
                : [sp] "+s"(sp), [td] "=&s"(td), "=&{s[96:97]}"(tt), "=&{v[112:115]}"(y)
                : [yv] "v"(y_vo), [p0] "v"(px_vo), [p1] "v"(px_vo1), [pm] "v"(pm_vo), [rs] "s"(c.rq), [sb] "s"(sb), [tq] "s"(tq), [w] "s"(w),
                  [ix] "v"(idx), [lw] "v"(rw_), [lr] "v"(rr_), [mv] "v"(m_vo), [ms] "s"(m_so), "{v[140:147]}"(u0), "{v[148:155]}"(u1)
                : "memory", "scc", "vcc", "v100", "v101", "v102", "v103", "v104", "v105", "v106", "v107", "v108", "v109", "v110",
                  "v116", "v117", "v118", "v119", "v120", "v121", "v122", "v123", "v124", "v125", "v126", "v127", "v128", "v129", "v130", "v131", "v132", "v133", "v134", "v135");
        }
        LPT(4, t_p);
        if (w < 4) {
            LMAX(s, 2, now32() - t_p);
            LMAX(s, 3, sp != sp0);
        } else if (w == 5) {
            const u32 t = now32() + T_PUB, due = t_p + T_RB + lv;
            const bool late = due - t > 1000u;
            hist = (hist << 1) | (t_p != t_nom && s > T_WARM ? 1u : 0u);
            if (__builtin_popcount(hist & ~(hist >> 1) & 0xFFu) >= 2 || (hist & 7u) == 7u) { lv = min(lv + 1, 15u); hist = 1; }
            else if ((hist & 0x3FFFu) == 0 && lv > 0) { --lv; hist = 0x100; }
            t_nom = due + T_D + 2 * lv;
            if ((late || r == 0) && lane == 0)
                __hip_atomic_fetch_min(c.fl + (F_TW * 64 + (s + 1) * 32), ~(((late ? t + (T_D + T_DS) + 2 * lv : t_nom) << 4) | lv), RLX_AGENT);
        }
        slot = slotn;
    }
    st4(y, c.rs, yl_vo, (u32)(NSEG - 2) * 327680u);
    DRAIN();
    BAR();
    if (w == 6) __hip_atomic_store(flag(c, F_YR + r), (u32)(NSEG - 2), RLX_AGENT);
    PDUMP(64 + r); LPD(64 + r);
}

DEV void role_px(const Params& P, const Ctx& c, int id, bool keys) {
    const int w = c.wave, lane = c.lane, h = (id & 3) + 4 * (w >> 2) + 8 * (id >> 2);
    lchar* ximg = c.lds;
    u32x8 w0[10], w1[10];
    const int j0 = keys ? 1 : 0;
    x_stage(c, P.x, j0 + (w >> 2), ximg + (w >> 2) * 40960, w & 3);
    u32x8 r0[10], r1[10];
    fetch_w<8192>(P, c, keys ? P.Wk : P.Wq, (u32)(h * 128 + 32 * (w & 3)) * 2 + 9 * KS, -5 * KS, -KS, r0, r1);
#pragma unroll
    for (int k = 0; k < 10; ++k) { w0[k] = r0[9 - k]; w1[k] = r1[9 - k]; }
    STAGED();
    gu32* fl = flag(c, (keys ? F_KX : F_QX) + 4 * h + (w & 3));
    const u32 o_vo = (keys ? OFF_KX : OFF_QX) + (u32)(h * 8 + (w & 3)) * 1024 + lane * 16;
    {
        u32x4 o[2];
        first_proj<true>(c, ximg, w0, w1, o);
#pragma unroll
        for (int tt = 0; tt < 2; ++tt) st4(o[tt], c.rs, o_vo + tt * 4096, (u32)j0 * 262144u);
        wait_armed(P, c);
        DRAIN();
        __hip_atomic_store(fl, (u32)j0, RLX_AGENT);
        if (w < 4) x_stage(c, P.x, j0 + 2, ximg + 2 * 40960, w);
    }
    int xb = 1;
    RSTAMP(1);
    BAR();
    for (int j = j0 + 1; j < NSEG; ++j) {
        u32x4 o[2];
        tok_proj<false, true>(c, ximg + xb * 40960, w0, w1, o, XDma(), fl, w < 4 || (keys && j > PX_LEAD) ? 99u : (u32)(j - 1));
        DRAIN();
        const bool paced = keys && j > PX_LEAD;
        if (paced) {
            if (w == 0) {
                u32 sp = SPIN_CAP, tw;
                while (__builtin_amdgcn_readfirstlane(tw = time_word(c, j - PX_LEAD)) == (int)SENT) { __builtin_amdgcn_s_sleep(40); SPIN_TICK(sp); }
                wait_until(mem_time(tw) + 36);
            }
            BAR();
        }
        __hip_atomic_store(fl, (u32)(j - 1), RLX_AGENT);
#pragma unroll
        for (int tt = 0; tt < 2; ++tt) st4(o[tt], c.rs, o_vo + tt * 4096, (u32)j * 262144u);
        if (w < 4) x_stage(c, P.x, j + 2, ximg + (xb == 0 ? 2 : xb - 1) * 40960, w);
        if (!paced && (w < 4 || j <= j0 + PX_NOW)) {
            if (w < 4) asm volatile("s_waitcnt vmcnt(10)" ::: "memory"); else DRAIN();
            __hip_atomic_store(fl, (u32)j, RLX_AGENT);
        }
        if (!paced) BAR();
        xb = xb == 2 ? 0 : xb + 1;
    }
    DRAIN();
    __hip_atomic_store(fl, (u32)(NSEG - 1), RLX_AGENT);
}

DEV void role_vox(const Params& P, const Ctx& c, int h) {
    const int w = c.wave, n = c.n, g = c.g, lane = c.lane;
    lchar* ximg = c.lds;
    lchar* vimg = c.lds + 122880;
    u32x8 w0[10], w1[10];
    x_stage(c, P.x, w >> 2, ximg + (w >> 2) * 40960, w & 3);
    if (w < 4) {
        fetch_w<8192>(P, c, P.Wv, (u32)(h * 128 + 32 * w) * 2, 5 * KS, KS, w0, w1);
        STAGED();
        u32x4 o[2];
        first_proj(c, ximg, w0, w1, o);
#pragma unroll
        for (int tt = 0; tt < 2; ++tt) *(LAS u32x4*)(vimg + (tt * 4 + w) * 1024 + lane * 16) = o[tt];
    } else {
        fetch_w<1280>(P, c, P.Wo, (u32)(h * 128 * 640 + 160 * (w - 4)) * 2, 64 * 1280, 64, w0, w1);
        STAGED();
#pragma unroll
        for (int q = 0; q < 10; ++q) split_pair(w0[q], w1[q]);
    }
    RSTAMP(1);
    if (w >= 4) { wait_armed(P, c); x_stage(c, P.x, 2, ximg + 2 * 40960, w - 4); }
    BAR();
    int xb = 1;
    const u32 u_vo = OFF_U + (u32)((10 * (w & 3) + (n >> 3)) * 32 + h) * 1024 + (u32)(g * 16 + (n & 7)) * 16;
    if (w >= 4) __builtin_amdgcn_s_setprio(2);
    gu32* f_u = flag(c, F_U + 32 * (w & 3) + h);
    PDECL;
    for (int i = 1; i <= NSEG; ++i) {
        const int s = i < 45 ? i : 0; (void)s;
        if (w < 4) {
            if (i < NSEG) {
                u32x4 o[2];
                tok_proj<false>(c, ximg + xb * 40960, w0, w1, o, XDma());
#pragma unroll
                for (int tt = 0; tt < 2; ++tt) *(LAS u32x4*)(vimg + (i & 1) * 8192 + (tt * 4 + w) * 1024 + lane * 16) = o[tt];
            }
        } else {
            u32x4 a[8];
#pragma unroll
            for (int k = 0; k < 8; ++k) a[k] = lds4(vimg + ((i - 1) & 1) * 8192 + k * 1024 + lane * 16);
            f32x16 da; f32x8 db;
            PIN("={v[228:243]}"(da), "={v[244:251]}"(db));
            const u32 uo = u_vo + (u32)(i - 1) * 1310720u;
            asm volatile("v_mfma_f32_16x16x32_bf16 v[228:231], %[a0], %[b0], 0\n\tv_mfma_f32_16x16x32_bf16 v[232:235], %[a4], %[b0], 0\n\tv_mfma_f32_16x16x32_bf16 v[228:231], %[a1], %[b1], v[228:231]\n\tv_mfma_f32_16x16x32_bf16 v[232:235], %[a5], %[b1], v[232:235]\n\tv_mfma_f32_16x16x32_bf16 v[228:231], %[a2], %[b2], v[228:231]\n\tv_mfma_f32_16x16x32_bf16 v[232:235], %[a6], %[b2], v[232:235]\n\tv_mfma_f32_16x16x32_bf16 v[228:231], %[a3], %[b3], v[228:231]\n\tv_mfma_f32_16x16x32_bf16 v[232:235], %[a7], %[b3], v[232:235]"
                         : "+{v[228:243]}"(da), "+{v[244:251]}"(db)
                         : [a0] "v"(a[0]), [a1] "v"(a[1]), [a2] "v"(a[2]), [a3] "v"(a[3]), [a4] "v"(a[4]), [a5] "v"(a[5]), [a6] "v"(a[6]), [a7] "v"(a[7]), [b0] "v"(lo4(w0[0])), [b1] "v"(hi4(w0[0])), [b2] "v"(lo4(w0[5])), [b3] "v"(hi4(w0[5])), [vo] "v"(uo), [rs] "s"(c.rq), [so] "s"(0u) : "memory", "v224", "v225", "v226", "v227");
            asm volatile("v_mfma_f32_16x16x32_bf16 v[236:239], %[a0], %[b0], 0\n\tv_mfma_f32_16x16x32_bf16 v[240:243], %[a4], %[b0], 0\n\tv_mfma_f32_16x16x32_bf16 v[236:239], %[a1], %[b1], v[236:239]\n\tv_mfma_f32_16x16x32_bf16 v[240:243], %[a5], %[b1], v[240:243]\n\tv_mfma_f32_16x16x32_bf16 v[236:239], %[a2], %[b2], v[236:239]\n\tv_mfma_f32_16x16x32_bf16 v[240:243], %[a6], %[b2], v[240:243]\n\tv_mfma_f32_16x16x32_bf16 v[236:239], %[a3], %[b3], v[236:239]\n\tv_mfma_f32_16x16x32_bf16 v[240:243], %[a7], %[b3], v[240:243]"
                         : "+{v[228:243]}"(da), "+{v[244:251]}"(db)
                         : [a0] "v"(a[0]), [a1] "v"(a[1]), [a2] "v"(a[2]), [a3] "v"(a[3]), [a4] "v"(a[4]), [a5] "v"(a[5]), [a6] "v"(a[6]), [a7] "v"(a[7]), [b0] "v"(lo4(w1[0])), [b1] "v"(hi4(w1[0])), [b2] "v"(lo4(w1[5])), [b3] "v"(hi4(w1[5])), [vo] "v"(uo), [rs] "s"(c.rq), [so] "s"(0u) : "memory", "v224", "v225", "v226", "v227");
            asm volatile("v_mfma_f32_16x16x32_bf16 v[244:247], %[a0], %[b0], 0\n\tv_mfma_f32_16x16x32_bf16 v[248:251], %[a4], %[b0], 0\n\tv_cvt_pk_bf16_f32 v224, v228, v229\n\tv_mfma_f32_16x16x32_bf16 v[244:247], %[a1], %[b1], v[244:247]\n\tv_cvt_pk_bf16_f32 v225, v232, v233\n\tv_mfma_f32_16x16x32_bf16 v[248:251], %[a5], %[b1], v[248:251]\n\tv_cvt_pk_bf16_f32 v226, v230, v231\n\tv_mfma_f32_16x16x32_bf16 v[244:247], %[a2], %[b2], v[244:247]\n\tv_cvt_pk_bf16_f32 v227, v234, v235\n\tv_mfma_f32_16x16x32_bf16 v[248:251], %[a6], %[b2], v[248:251]\n\tbuffer_store_dwordx4 v[224:227], %[vo], %[rs], %[so] offen offset:0 sc1\n\tv_mfma_f32_16x16x32_bf16 v[244:247], %[a3], %[b3], v[244:247]\n\tv_mfma_f32_16x16x32_bf16 v[248:251], %[a7], %[b3], v[248:251]"
                         : "+{v[228:243]}"(da), "+{v[244:251]}"(db)
                         : [a0] "v"(a[0]), [a1] "v"(a[1]), [a2] "v"(a[2]), [a3] "v"(a[3]), [a4] "v"(a[4]), [a5] "v"(a[5]), [a6] "v"(a[6]), [a7] "v"(a[7]), [b0] "v"(lo4(w0[1])), [b1] "v"(hi4(w0[1])), [b2] "v"(lo4(w0[6])), [b3] "v"(hi4(w0[6])), [vo] "v"(uo), [rs] "s"(c.rq), [so] "s"(0u) : "memory", "v224", "v225", "v226", "v227");
            asm volatile("v_mfma_f32_16x16x32_bf16 v[228:231], %[a0], %[b0], 0\n\tv_mfma_f32_16x16x32_bf16 v[232:235], %[a4], %[b0], 0\n\tv_cvt_pk_bf16_f32 v224, v236, v237\n\tv_mfma_f32_16x16x32_bf16 v[228:231], %[a1], %[b1], v[228:231]\n\tv_cvt_pk_bf16_f32 v225, v240, v241\n\tv_mfma_f32_16x16x32_bf16 v[232:235], %[a5], %[b1], v[232:235]\n\tv_cvt_pk_bf16_f32 v226, v238, v239\n\tv_mfma_f32_16x16x32_bf16 v[228:231], %[a2], %[b2], v[228:231]\n\tv_cvt_pk_bf16_f32 v227, v242, v243\n\tv_mfma_f32_16x16x32_bf16 v[232:235], %[a6], %[b2], v[232:235]\n\tbuffer_store_dwordx4 v[224:227], %[vo], %[rs], %[so] offen offset:128 sc1\n\tv_mfma_f32_16x16x32_bf16 v[228:231], %[a3], %[b3], v[228:231]\n\tv_mfma_f32_16x16x32_bf16 v[232:235], %[a7], %[b3], v[232:235]"
                         : "+{v[228:243]}"(da), "+{v[244:251]}"(db)
                         : [a0] "v"(a[0]), [a1] "v"(a[1]), [a2] "v"(a[2]), [a3] "v"(a[3]), [a4] "v"(a[4]), [a5] "v"(a[5]), [a6] "v"(a[6]), [a7] "v"(a[7]), [b0] "v"(lo4(w1[1])), [b1] "v"(hi4(w1[1])), [b2] "v"(lo4(w1[6])), [b3] "v"(hi4(w1[6])), [vo] "v"(uo), [rs] "s"(c.rq), [so] "s"(0u) : "memory", "v224", "v225", "v226", "v227");
            asm volatile("s_waitcnt vmcnt(2)" ::: "memory");
            if (i > VX_NOW + 1) __hip_atomic_store(f_u, (u32)(i - 2), RLX_AGENT);
            asm volatile("v_mfma_f32_16x16x32_bf16 v[236:239], %[a0], %[b0], 0\n\tv_mfma_f32_16x16x32_bf16 v[240:243], %[a4], %[b0], 0\n\tv_cvt_pk_bf16_f32 v224, v244, v245\n\tv_mfma_f32_16x16x32_bf16 v[236:239], %[a1], %[b1], v[236:239]\n\tv_cvt_pk_bf16_f32 v225, v248, v249\n\tv_mfma_f32_16x16x32_bf16 v[240:243], %[a5], %[b1], v[240:243]\n\tv_cvt_pk_bf16_f32 v226, v246, v247\n\tv_mfma_f32_16x16x32_bf16 v[236:239], %[a2], %[b2], v[236:239]\n\tv_cvt_pk_bf16_f32 v227, v250, v251\n\tv_mfma_f32_16x16x32_bf16 v[240:243], %[a6], %[b2], v[240:243]\n\tbuffer_store_dwordx4 v[224:227], %[vo], %[rs], %[so] offen offset:0 sc1\n\tv_mfma_f32_16x16x32_bf16 v[236:239], %[a3], %[b3], v[236:239]\n\tv_mfma_f32_16x16x32_bf16 v[240:243], %[a7], %[b3], v[240:243]"
                         : "+{v[228:243]}"(da), "+{v[244:251]}"(db)
                         : [a0] "v"(a[0]), [a1] "v"(a[1]), [a2] "v"(a[2]), [a3] "v"(a[3]), [a4] "v"(a[4]), [a5] "v"(a[5]), [a6] "v"(a[6]), [a7] "v"(a[7]), [b0] "v"(lo4(w0[2])), [b1] "v"(hi4(w0[2])), [b2] "v"(lo4(w0[7])), [b3] "v"(hi4(w0[7])), [vo] "v"(uo), [rs] "s"(c.rq), [so] "s"(65536u) : "memory", "v224", "v225", "v226", "v227");
            asm volatile("v_mfma_f32_16x16x32_bf16 v[244:247], %[a0], %[b0], 0\n\tv_mfma_f32_16x16x32_bf16 v[248:251], %[a4], %[b0], 0\n\tv_cvt_pk_bf16_f32 v224, v228, v229\n\tv_mfma_f32_16x16x32_bf16 v[244:247], %[a1], %[b1], v[244:247]\n\tv_cvt_pk_bf16_f32 v225, v232, v233\n\tv_mfma_f32_16x16x32_bf16 v[248:251], %[a5], %[b1], v[248:251]\n\tv_cvt_pk_bf16_f32 v226, v230, v231\n\tv_mfma_f32_16x16x32_bf16 v[244:247], %[a2], %[b2], v[244:247]\n\tv_cvt_pk_bf16_f32 v227, v234, v235\n\tv_mfma_f32_16x16x32_bf16 v[248:251], %[a6], %[b2], v[248:251]\n\tbuffer_store_dwordx4 v[224:227], %[vo], %[rs], %[so] offen offset:128 sc1\n\tv_mfma_f32_16x16x32_bf16 v[244:247], %[a3], %[b3], v[244:247]\n\tv_mfma_f32_16x16x32_bf16 v[248:251], %[a7], %[b3], v[248:251]"
                         : "+{v[228:243]}"(da), "+{v[244:251]}"(db)
                         : [a0] "v"(a[0]), [a1] "v"(a[1]), [a2] "v"(a[2]), [a3] "v"(a[3]), [a4] "v"(a[4]), [a5] "v"(a[5]), [a6] "v"(a[6]), [a7] "v"(a[7]), [b0] "v"(lo4(w1[2])), [b1] "v"(hi4(w1[2])), [b2] "v"(lo4(w1[7])), [b3] "v"(hi4(w1[7])), [vo] "v"(uo), [rs] "s"(c.rq), [so] "s"(65536u) : "memory", "v224", "v225", "v226", "v227");
            asm volatile("v_mfma_f32_16x16x32_bf16 v[228:231], %[a0], %[b0], 0\n\tv_mfma_f32_16x16x32_bf16 v[232:235], %[a4], %[b0], 0\n\tv_cvt_pk_bf16_f32 v224, v236, v237\n\tv_mfma_f32_16x16x32_bf16 v[228:231], %[a1], %[b1], v[228:231]\n\tv_cvt_pk_bf16_f32 v225, v240, v241\n\tv_mfma_f32_16x16x32_bf16 v[232:235], %[a5], %[b1], v[232:235]\n\tv_cvt_pk_bf16_f32 v226, v238, v239\n\tv_mfma_f32_16x16x32_bf16 v[228:231], %[a2], %[b2], v[228:231]\n\tv_cvt_pk_bf16_f32 v227, v242, v243\n\tv_mfma_f32_16x16x32_bf16 v[232:235], %[a6], %[b2], v[232:235]\n\tbuffer_store_dwordx4 v[224:227], %[vo], %[rs], %[so] offen offset:0 sc1\n\tv_mfma_f32_16x16x32_bf16 v[228:231], %[a3], %[b3], v[228:231]\n\tv_mfma_f32_16x16x32_bf16 v[232:235], %[a7], %[b3], v[232:235]"
                         : "+{v[228:243]}"(da), "+{v[244:251]}"(db)
                         : [a0] "v"(a[0]), [a1] "v"(a[1]), [a2] "v"(a[2]), [a3] "v"(a[3]), [a4] "v"(a[4]), [a5] "v"(a[5]), [a6] "v"(a[6]), [a7] "v"(a[7]), [b0] "v"(lo4(w0[3])), [b1] "v"(hi4(w0[3])), [b2] "v"(lo4(w0[8])), [b3] "v"(hi4(w0[8])), [vo] "v"(uo), [rs] "s"(c.rq), [so] "s"(131072u) : "memory", "v224", "v225", "v226", "v227");
            asm volatile("v_mfma_f32_16x16x32_bf16 v[236:239], %[a0], %[b0], 0\n\tv_mfma_f32_16x16x32_bf16 v[240:243], %[a4], %[b0], 0\n\tv_cvt_pk_bf16_f32 v224, v244, v245\n\tv_mfma_f32_16x16x32_bf16 v[236:239], %[a1], %[b1], v[236:239]\n\tv_cvt_pk_bf16_f32 v225, v248, v249\n\tv_mfma_f32_16x16x32_bf16 v[240:243], %[a5], %[b1], v[240:243]\n\tv_cvt_pk_bf16_f32 v226, v246, v247\n\tv_mfma_f32_16x16x32_bf16 v[236:239], %[a2], %[b2], v[236:239]\n\tv_cvt_pk_bf16_f32 v227, v250, v251\n\tv_mfma_f32_16x16x32_bf16 v[240:243], %[a6], %[b2], v[240:243]\n\tbuffer_store_dwordx4 v[224:227], %[vo], %[rs], %[so] offen offset:128 sc1\n\tv_mfma_f32_16x16x32_bf16 v[236:239], %[a3], %[b3], v[236:239]\n\tv_mfma_f32_16x16x32_bf16 v[240:243], %[a7], %[b3], v[240:243]"
                         : "+{v[228:243]}"(da), "+{v[244:251]}"(db)
                         : [a0] "v"(a[0]), [a1] "v"(a[1]), [a2] "v"(a[2]), [a3] "v"(a[3]), [a4] "v"(a[4]), [a5] "v"(a[5]), [a6] "v"(a[6]), [a7] "v"(a[7]), [b0] "v"(lo4(w1[3])), [b1] "v"(hi4(w1[3])), [b2] "v"(lo4(w1[8])), [b3] "v"(hi4(w1[8])), [vo] "v"(uo), [rs] "s"(c.rq), [so] "s"(131072u) : "memory", "v224", "v225", "v226", "v227");
            asm volatile("v_mfma_f32_16x16x32_bf16 v[244:247], %[a0], %[b0], 0\n\tv_mfma_f32_16x16x32_bf16 v[248:251], %[a4], %[b0], 0\n\tv_cvt_pk_bf16_f32 v224, v228, v229\n\tv_mfma_f32_16x16x32_bf16 v[244:247], %[a1], %[b1], v[244:247]\n\tv_cvt_pk_bf16_f32 v225, v232, v233\n\tv_mfma_f32_16x16x32_bf16 v[248:251], %[a5], %[b1], v[248:251]\n\tv_cvt_pk_bf16_f32 v226, v230, v231\n\tv_mfma_f32_16x16x32_bf16 v[244:247], %[a2], %[b2], v[244:247]\n\tv_cvt_pk_bf16_f32 v227, v234, v235\n\tv_mfma_f32_16x16x32_bf16 v[248:251], %[a6], %[b2], v[248:251]\n\tbuffer_store_dwordx4 v[224:227], %[vo], %[rs], %[so] offen offset:0 sc1\n\tv_mfma_f32_16x16x32_bf16 v[244:247], %[a3], %[b3], v[244:247]\n\tv_mfma_f32_16x16x32_bf16 v[248:251], %[a7], %[b3], v[248:251]"
                         : "+{v[228:243]}"(da), "+{v[244:251]}"(db)
                         : [a0] "v"(a[0]), [a1] "v"(a[1]), [a2] "v"(a[2]), [a3] "v"(a[3]), [a4] "v"(a[4]), [a5] "v"(a[5]), [a6] "v"(a[6]), [a7] "v"(a[7]), [b0] "v"(lo4(w0[4])), [b1] "v"(hi4(w0[4])), [b2] "v"(lo4(w0[9])), [b3] "v"(hi4(w0[9])), [vo] "v"(uo), [rs] "s"(c.rq), [so] "s"(196608u) : "memory", "v224", "v225", "v226", "v227");
            asm volatile("v_mfma_f32_16x16x32_bf16 v[228:231], %[a0], %[b0], 0\n\tv_mfma_f32_16x16x32_bf16 v[232:235], %[a4], %[b0], 0\n\tv_cvt_pk_bf16_f32 v224, v236, v237\n\tv_mfma_f32_16x16x32_bf16 v[228:231], %[a1], %[b1], v[228:231]\n\tv_cvt_pk_bf16_f32 v225, v240, v241\n\tv_mfma_f32_16x16x32_bf16 v[232:235], %[a5], %[b1], v[232:235]\n\tv_cvt_pk_bf16_f32 v226, v238, v239\n\tv_mfma_f32_16x16x32_bf16 v[228:231], %[a2], %[b2], v[228:231]\n\tv_cvt_pk_bf16_f32 v227, v242, v243\n\tv_mfma_f32_16x16x32_bf16 v[232:235], %[a6], %[b2], v[232:235]\n\tbuffer_store_dwordx4 v[224:227], %[vo], %[rs], %[so] offen offset:128 sc1\n\tv_mfma_f32_16x16x32_bf16 v[228:231], %[a3], %[b3], v[228:231]\n\tv_mfma_f32_16x16x32_bf16 v[232:235], %[a7], %[b3], v[232:235]"
                         : "+{v[228:243]}"(da), "+{v[244:251]}"(db)
                         : [a0] "v"(a[0]), [a1] "v"(a[1]), [a2] "v"(a[2]), [a3] "v"(a[3]), [a4] "v"(a[4]), [a5] "v"(a[5]), [a6] "v"(a[6]), [a7] "v"(a[7]), [b0] "v"(lo4(w1[4])), [b1] "v"(hi4(w1[4])), [b2] "v"(lo4(w1[9])), [b3] "v"(hi4(w1[9])), [vo] "v"(uo), [rs] "s"(c.rq), [so] "s"(196608u) : "memory", "v224", "v225", "v226", "v227");
            asm volatile("v_cvt_pk_bf16_f32 v224, v244, v245\n\tv_cvt_pk_bf16_f32 v225, v248, v249\n\tv_cvt_pk_bf16_f32 v226, v246, v247\n\tv_cvt_pk_bf16_f32 v227, v250, v251\n\tbuffer_store_dwordx4 v[224:227], %[vo], %[rs], %[so] offen offset:0 sc1\n\ts_nop 2\n\tv_cvt_pk_bf16_f32 v224, v228, v229\n\tv_cvt_pk_bf16_f32 v225, v232, v233\n\tv_cvt_pk_bf16_f32 v226, v230, v231\n\tv_cvt_pk_bf16_f32 v227, v234, v235\n\tbuffer_store_dwordx4 v[224:227], %[vo], %[rs], %[so] offen offset:128 sc1\n\ts_nop 1"
                         : "+{v[228:243]}"(da), "+{v[244:251]}"(db)
                         : [vo] "v"(uo), [rs] "s"(c.rq), [so] "s"(262144u) : "memory", "v224", "v225", "v226", "v227");
        }
        PMARK(1);
        if (w >= 4) {
            x_stage(c, P.x, i + 2, ximg + (xb == 0 ? 2 : xb - 1) * 40960, w - 4);
            if (i <= VX_NOW || i == NSEG) {
                asm volatile("s_waitcnt vmcnt(10)" ::: "memory");
                __hip_atomic_store(f_u, (u32)(i - 1), RLX_AGENT);
            }
        }
        xb = xb == 2 ? 0 : xb + 1;
        PMARK(2);
        BAR();
        PMARK(3);
        if (i > 0) { STAMP(224 + h, i - 1, 0); }
    }
    DRAIN();
    PDUMP(104 + h);
}

DEV void wrk_barrier(const Ctx& c, int fi, int arrivals, bool arrive) {
    DRAIN();
    BAR();
    if (c.wave == 0) {
        if (c.lane == 0 && arrive) __hip_atomic_fetch_add(flag(c, fi), 1u, RLX_AGENT);
        wait_ge<16>(c, fi, arrivals - 1);
    }
    BAR();
}

DEV void side_jobs(const Params& P, const Ctx& c, int wid) {
    const int n = c.n, g = c.g;
    const rsrc_t rw = mk_rsrc(P.wo, 640 * 640 * 2), rx = mk_rsrc(P.x, 2048 * 640 * 2);
#pragma unroll
    for (int i = 0; i < 64 / NFB; ++i) {
        const int t = wid + 8 * NFB * i, cg = t / 24, ks = t % 24, ga = 4 * ks + g, hd = ga / 3, u = ga % 3;
        u32 d[8];
#pragma unroll
        for (int j = 0; j < 8; ++j) {
            const int dd = u < 2 ? 4 * u + (j & 3) + 8 * (j >> 2) : 16 + j;
            d[j] = pld1(rw, dd < 20 && t < 480 ? (u32)((20 * hd + dd) * 640 + 32 * cg + 2 * n) * 2 : 0x80000000u, 0);
        }
        u32x4 a, b;
#pragma unroll
        for (int j = 0; j < 4; ++j) { a[j] = __builtin_amdgcn_perm(d[2 * j + 1], d[2 * j], 0x05040100); b[j] = __builtin_amdgcn_perm(d[2 * j + 1], d[2 * j], 0x07060302); }
        const u32 o = t < 480 ? OFF_WOF + (u32)((2 * cg + (n >> 3)) * 24 + ks) * 1024 + (u32)(g * 16 + (n & 7)) * 16 : 0x80000000u;
        st4(a, c.rs, o, 0);
        st4(b, c.rs, o + 128, 0);
    }
    u32x4 gv[5];
#pragma unroll
    for (int k = 0; k < 5; ++k) gv[k] = *(const GAS u32x4*)(P.gw + 8 * (n + 16 * k));
#pragma unroll
    for (int i = 0; i < 64 / NFB; ++i) {
        const int t = (wid + 8 * NFB * i) * 4 + g;
        float s = 0.f;
#pragma unroll
        for (int k = 0; k < 5; ++k) {
            const u32x4 xv = pld4(rx, (u32)t * 1280 + (u32)(n + 16 * k) * 16, 0);
#pragma unroll
            for (int j = 0; j < 4; ++j) {
                const u32 pr = pk(bf_lo(xv[j]) * bf_lo(gv[k][j]), bf_hi(xv[j]) * bf_hi(gv[k][j]));
                s += bf_lo(pr) + bf_hi(pr);
            }
        }
#pragma unroll
        for (int o = 8; o; o >>= 1) s += __shfl_xor(s, o);
        st1(__float_as_uint(1.0f / (1.0f + __expf(-s))), c.rs, n == 0 && t < 2048 ? OFF_G + (u32)t * 4 : 0x80000000u, 0);
    }
}
DEV void wrk_qkv(const Params& P, const Ctx& c, int wk) {
    const int w = c.wave, n = c.n, g = c.g;
    const int G60 = 8 * (wk & 7) + w, rr = wk >> 3;
    const int s0 = 8 * rr, s1 = s0 + 8;
    const int kind = G60 / 20, G = G60 % 20;
    const int cp = 16 * G + n;
    u32 eo, es;
    if (kind < 2) {
        const int go = 4 * G + g, pr = go / 5, ko = go % 5;
        eo = ko != 2 ? OFF_Q0 + (u32)(kind * 32 + 2 * pr + (ko > 2)) * 65536 + (u32)((ko % 3) * 32 + n) * 16
                     : OFF_Q1 + (u32)(kind * 16 + pr) * 32768 + (u32)n * 16;
        es = ko != 2 ? 1024 : 512;
    } else {
        eo = OFF_VT + (u32)((g & 1) * 2 + (g >> 1)) * 5120 + (u32)cp * 16;
        es = 40960;
    }
    u32x8 w0[10], w1[10];
    if (w < 4) x_stage(c, P.x, s0, c.lds + (s0 & 1) * 40960, w);
    fetch_w<3840>(P, c, P.wqkv, (u32)min(G60, 59) * 64, 5 * 64 * 3840, 64 * 3840, w0, w1);
#pragma unroll
    for (int k = 0; k < 10; ++k) split_pair(w0[k], w1[k]);
    lchar* ximg = c.lds;
    RSTAMP(1);
    DRAIN();
    BAR();
    const float qs = kind == 0 ? 0.22360679774997896f * LOG2E : 1.0f;
    for (int s = s0; s < s1; ++s) {
        const XDma dm = x_dma(c, P.x, s + 1, ximg + ((s + 1) & 1) * 40960, w & 3);
        f32x4 acc[2][2];
        if (kind < 2) {
            if (w < 4) tok_acc<true, false, false>(c, ximg + (s & 1) * 40960, w0, w1, acc, dm, nullptr, 0);
            else tok_acc<false, false, false>(c, ximg + (s & 1) * 40960, w0, w1, acc, dm, nullptr, 0);
#pragma unroll
            for (int tt = 0; tt < 2; ++tt)
                st4((u32x4){pk(acc[tt][0][0] * qs, acc[tt][1][0] * qs), pk(acc[tt][0][1] * qs, acc[tt][1][1] * qs),
                            pk(acc[tt][0][2] * qs, acc[tt][1][2] * qs), pk(acc[tt][0][3] * qs, acc[tt][1][3] * qs)}, c.rs, eo + (u32)s * es + tt * 256, 0);
        } else if (kind == 2) {
            if (w < 4) tok_acc<true, false, true>(c, ximg + (s & 1) * 40960, w0, w1, acc, dm, nullptr, 0);
            else tok_acc<false, false, true>(c, ximg + (s & 1) * 40960, w0, w1, acc, dm, nullptr, 0);
#pragma unroll
            for (int tt = 0; tt < 2; ++tt) {
                auto r0 = __builtin_amdgcn_permlane32_swap(pk(acc[tt][0][0], acc[tt][0][1]), pk(acc[tt][1][0], acc[tt][1][1]), false, false);
                auto r1 = __builtin_amdgcn_permlane32_swap(pk(acc[tt][0][2], acc[tt][0][3]), pk(acc[tt][1][2], acc[tt][1][3]), false, false);
                st4((u32x4){r0[0], r1[0], r0[1], r1[1]}, c.rs, eo + (u32)s * es + tt * 20480, 0);
            }
        }
        asm volatile("s_waitcnt vmcnt(2)" ::: "memory");
        BAR();
    }
}

constexpr float RESCALE_THR = 6.0f;
DEV float row_max16(const f32x16& s) {
    const float a = fmx(fmx(fmx(fmx(s[0], s[1]), s[2]), fmx(fmx(s[3], s[4]), s[5])), fmx(fmx(s[6], s[7]), s[8]));
    const float b = fmx(fmx(fmx(s[9], s[10]), s[11]), fmx(fmx(s[12], s[13]), s[14]));
    return fmx(fmx(a, b), s[15]);
}
template <int OFF>
DEV void v_read(u32x4& a, u32x4& b, lchar* p) {
    asm volatile("s_mov_b64 exec, %3\n\tds_read_b128 %0, %2 offset:%4\n\tds_read_b128 %1, %2 offset:%5\n\ts_mov_b64 exec, -1"
                 : "+v"(a), "+v"(b) : "v"(p), "s"(0x000FFFFF000FFFFFull), "i"(OFF), "i"(OFF + 640) : "memory");
}
template <int ODD, bool CHECK>
DEV void attn_step(lchar* vp, lchar* k0p, lchar* k1p, const u32x4& q0, const u32x4& q1, u32x4& k0, u32x4& k1, const u32x4& v0, const u32x4& v1,
                   u32x4& vn0, u32x4& vn1, const f32x16& cur, f32x16& nxt, f32x16& cm, f32x16& o, bool last = false) {
    float e[16];
    u32 p[8];
    nxt = mma32(k0, q0, cm);
    SB();
    k0 = lds4(k0p + (2 + ODD) * 1024);
#pragma unroll
    for (int i = 0; i < 4; ++i) e[i] = __builtin_amdgcn_exp2f(cur[i]);
    SB();
    nxt = mma32(k1, q1, nxt);
    SB();
    v_read<(1 + ODD) * 1280>(vn0, vn1, vp);
    { const u32x2 t = lds2(k1p + (2 + ODD) * 256); k1.x = t.x; k1.y = t.y; }
#pragma unroll
    for (int i = 4; i < 8; ++i) e[i] = __builtin_amdgcn_exp2f(cur[i]);
#pragma unroll
    for (int i = 0; i < 4; ++i) p[i] = pk(e[2 * i], e[2 * i + 1]);
#pragma unroll
    for (int i = 8; i < 10; ++i) e[i] = __builtin_amdgcn_exp2f(cur[i]);
    SB();
    o = mma32(v0, (u32x4){p[0], p[1], p[2], p[3]}, o);
    SB();
#pragma unroll
    for (int i = 10; i < 16; ++i) e[i] = __builtin_amdgcn_exp2f(cur[i]);
#pragma unroll
    for (int i = 4; i < 8; ++i) p[i] = pk(e[2 * i], e[2 * i + 1]);
    const float m0 = fmx(fmx(nxt[0], nxt[1]), nxt[2]), m1 = fmx(fmx(nxt[3], nxt[4]), nxt[5]);
    SB();
    o = mma32(v1, (u32x4){p[4], p[5], p[6], p[7]}, o);
    SB();
    if (!CHECK) return;
    const float pm = fmx(fmx(fmx(fmx(m0, m1), fmx(fmx(nxt[6], nxt[7]), nxt[8])), fmx(fmx(fmx(nxt[9], nxt[10]), nxt[11]), fmx(fmx(nxt[12], nxt[13]), nxt[14]))), nxt[15]);
    if (__builtin_expect(__builtin_amdgcn_ballot_w64(!(pm <= RESCALE_THR)) != 0 && !last, 0)) {
        const float dl = fmx(max_x32(pm), 0.f);
        const float f = __builtin_amdgcn_exp2f(-dl);
#pragma unroll
        for (int i = 0; i < 16; ++i) { nxt[i] -= dl; cm[i] -= dl; o[i] *= f; }
    }
}
template <bool CHECK>
DEV void attn_task(const Ctx& c, const u32x4& q0, const u32x4& q1, f32x16& o) {
    const int lane = c.lane, q32 = lane & 31, hh = lane >> 5;
    lchar* k0p = c.lds + lane * 16;
    lchar* k1p = c.lds + 65536 + q32 * 8;
    lchar* vp = c.lds + 81920 + (hh * 20 + min(q32, 19)) * 16;
    f32x16 cm, sa, sb;
#pragma unroll
    for (int i = 0; i < 16; ++i) { o[i] = 0.f; cm[i] = 0.f; }
    u32x4 va0 = {0x3F803F80u, 0x3F803F80u, 0x3F803F80u, 0x3F803F80u}, va1 = va0, vb0 = va0, vb1 = va0;
    v_read<0>(va0, va1, vp);
    u32x4 k0 = lds4(k0p), k1 = {0u, 0u, 0u, 0u};
    { const u32x2 t = lds2(k1p); k1.x = t.x; k1.y = t.y; }
    sa = mma32(k0, q0, cm);
    sa = mma32(k1, q1, sa);
    SB();
    k0 = lds4(k0p + 1024);
    { const u32x2 t = lds2(k1p + 256); k1.x = t.x; k1.y = t.y; }
    {
        const float dl = max_x32(row_max16(sa));
#pragma unroll
        for (int i = 0; i < 16; ++i) { sa[i] -= dl; cm[i] -= dl; }
    }
    for (int t = 0; t < 64; t += 2) {
        PIN("+v"(vp), "+v"(k0p), "+v"(k1p), "+v"(k1));
        attn_step<0, CHECK>(vp, k0p, k1p, q0, q1, k0, k1, va0, va1, vb0, vb1, sa, sb, cm, o);
        attn_step<1, CHECK>(vp, k0p, k1p, q0, q1, k0, k1, vb0, vb1, va0, va1, sb, sa, cm, o, t == 62);
        vp += 2560; k0p += 2048; k1p += 512;
    }
    asm volatile("s_waitcnt lgkmcnt(0)" : "+v"(va0), "+v"(va1) :: "memory");
}
DEV void wrk_attn(const Params& P, const Ctx& c, int wk, gu32* ticket) {
    const int lane = c.lane, tid = c.tid, h = wk >> 1;
    const int q32 = lane & 31, hh = lane >> 5;
#pragma unroll
    for (int r = 0; r < 2; ++r) {
        u32x4 t[4]; u32x2 t1[2]; u32x4 tv[5];
#pragma unroll
        for (int k = 0; k < 4; ++k) t[k] = ld4(c.rs, OFF_Q0 + tid * 16, (u32)(32 + h) * 65536 + (u32)(4 * r + k) * 8192);
#pragma unroll
        for (int k = 0; k < 2; ++k) t1[k] = ld2(c.rs, OFF_Q1 + tid * 16, (u32)(16 + (h >> 1)) * 32768 + (u32)(2 * r + k) * 8192 + (h & 1) * 8);
#pragma unroll
        for (int k = 0; k < 5; ++k) {
            const int v = tid + 512 * (5 * r + k), sh = v / 20, d = v % 20;
            tv[k] = ld4(c.rs, OFF_VT + (u32)(sh * 2 + (d & 1)) * 5120 + (u32)(d >> 1) * 16, (u32)h * 160);
        }
#pragma unroll
        for (int k = 0; k < 4; ++k) *(LAS u32x4*)(c.lds + (4 * r + k) * 8192 + tid * 16) = t[k];
#pragma unroll
        for (int k = 0; k < 2; ++k) *(LAS u32x2*)(c.lds + 65536 + (2 * r + k) * 4096 + tid * 8) = t1[k];
#pragma unroll
        for (int k = 0; k < 5; ++k) *(LAS u32x4*)(c.lds + 81920 + (5 * r + k) * 8192 + tid * 16) = tv[k];
    }
    BAR();
    int task = c.wave;
    while (task < 32) {
        const int qt = (wk & 1) * 32 + task;
        if (c.wave >= 4 && task >= AT_PRIO) __builtin_amdgcn_s_setprio(1);
        const u32x4 q0 = ld4(c.rs, OFF_Q0 + lane * 16, (u32)(h * 64 + qt) * 1024);
        const u32x2 q1h = ld2(c.rs, OFF_Q1 + q32 * 16, (u32)((h >> 1) * 2048 + qt * 32) * 16 + (h & 1) * 8);
        const u32x4 q1 = {hh ? 0u : q1h.x, hh ? 0u : q1h.y, 0u, 0u};
        f32x16 o;
        attn_task<false>(c, q0, q1, o);
        float big = 0.f;
#pragma unroll
        for (int i = 0; i < 12; ++i) big = fmx(big, __builtin_fabsf(o[i]));
        if (__builtin_amdgcn_ballot_w64(!(big < 1.0e30f)) != 0) attn_task<true>(c, q0, q1, o);
        u32 nx = 0;
        if (lane == 0) nx = __hip_atomic_fetch_add(ticket, 1u, RLX_AGENT);
        auto lr = __builtin_amdgcn_permlane32_swap(__float_as_uint(o[8]), __float_as_uint(o[8]), false, false);
        const float inv = 1.0f / __uint_as_float(lr[1]);
        const u32 ob = OFF_OA + (u32)(qt * 2 + (q32 >> 4)) * 24576 + (u32)(q32 & 15) * 16;
        st4((u32x4){pk(o[0] * inv, o[1] * inv), pk(o[2] * inv, o[3] * inv), pk(o[4] * inv, o[5] * inv), pk(o[6] * inv, o[7] * inv)}, c.rs, ob + (u32)(3 * h + hh) * 256, 0);
        st4((u32x4){pk(o[8] * inv, o[9] * inv), pk(o[10] * inv, o[11] * inv), 0u, 0u}, c.rs, hh ? 0x80000000u : ob + (u32)(3 * h + 2) * 256, 0);
        task = 9 + (int)__builtin_amdgcn_readfirstlane(nx);
    }
}

DEV void sum_b(const Ctx& c, int me) {
    const int w = c.wave, n = c.n, lane = c.lane;
    int prog = -1, done = -1;
    for (int t = me; t < 2 * S_SPLIT; t += NFB) {
        const int s = t >> 1, cg0 = 5 * (t & 1);
        u32 sp = SPIN_CAP;
        while (prog < s) {
            FENCE();
            int v = (int)__hip_atomic_load(flag(c, lane < 20 ? F_U + 32 * (lane >> 2) + 4 * w + (lane & 3) : F_YR + min(lane - 20, 39)), RLX_AGENT);
#pragma unroll
            for (int o = 32; o; o >>= 1) v = min(v, __shfl_xor(v, o));
            prog = __builtin_amdgcn_readfirstlane(v);
            if (prog < s) __builtin_amdgcn_s_sleep(12);
            SPIN_TICK(sp);
        }
        u32x4 b[5][2], a[5][4];
        float gt[2];
#pragma unroll
        for (int tt = 0; tt < 2; ++tt) gt[tt] = 1.0f - __uint_as_float(ld1(c.rs, OFF_G + (u32)(16 * tt + n) * 4, (u32)s * 128));
#pragma unroll
        for (int k = 0; k < 5; ++k)
#pragma unroll
            for (int tt = 0; tt < 2; ++tt)
                b[k][tt] = ld4(c.rs, OFF_PT + (k < 4 ? (u32)(tt * 32 + 4 * w + k) * 1024 : 65536u + (u32)(tt * 8 + w) * 1024) + lane * 16, (u32)s * 81920u);
        auto fetch = [&](int cg, int k) {
#pragma unroll
            for (int ct = 0; ct < 4; ++ct)
                a[k][ct] = k < 4 ? ld4(c.rs, OFF_U + (u32)(ct * 32 + 4 * w + k) * 1024 + lane * 16, (u32)s * 1310720u + (u32)cg * 131072u)
                                 : ld4(c.rs, OFF_YL + (u32)(ct * 8 + w) * 1024 + lane * 16, (u32)s * 327680u + (u32)cg * 32768u);
        };
#pragma unroll
        for (int k = 0; k < 5; ++k) fetch(cg0, k);
        SB();
#pragma unroll 1
        for (int i5 = 0; i5 < 5; ++i5) {
            f32x4 acc[2][4];
#pragma unroll
            for (int i = 0; i < 8; ++i) acc[i >> 2][i & 3] = ZERO4;
            const int cgn = i5 < 4 ? cg0 + i5 + 1 : 16000;
#pragma unroll
            for (int k = 0; k < 5; ++k) {
#pragma unroll
                for (int i = 0; i < 8; ++i) acc[i >> 2][i & 3] = mma16(a[k][i & 3], b[k][i >> 2], acc[i >> 2][i & 3]);
                PIN("+v"(acc[0][0]), "+v"(acc[0][1]), "+v"(acc[0][2]), "+v"(acc[0][3]), "+v"(acc[1][0]), "+v"(acc[1][1]), "+v"(acc[1][2]), "+v"(acc[1][3]));
                fetch(cgn, k);
                SB();
            }
            BAR();
            if (i5 == 0 && c.tid == 0) __hip_atomic_store(flag(c, F_RB + me), (u32)done, RLX_AGENT);
#pragma unroll
            for (int i = 0; i < 8; ++i) *(LAS f32x4*)(c.lds + (w * 8 + i) * 1024 + lane * 16) = acc[i >> 2][i & 3] * gt[i >> 2];
            BAR();
            f32x4 r = *(LAS f32x4*)(c.lds + w * 1024 + lane * 16);
#pragma unroll
            for (int i = 1; i < 8; ++i) r += *(LAS f32x4*)(c.lds + (i * 8 + w) * 1024 + lane * 16);
            st4(__builtin_bit_cast(u32x4, r), c.rs, OFF_RB + (u32)w * 1024 + lane * 16, (u32)(s * 10 + cg0 + i5) * 8192);
        }
        ++done;
    }
    DRAIN();
    BAR();
    if (c.tid == 0) __hip_atomic_store(flag(c, F_RB + me), (u32)done, RLX_AGENT);
}

DEV void out_store(const Params& P, const Ctx& c, const f32x4& t, int tt, int ct) {
    auto e0 = __builtin_amdgcn_permlane32_swap(__float_as_uint(t[0]), __float_as_uint(t[2]), false, false);
    auto e1 = __builtin_amdgcn_permlane32_swap(__float_as_uint(t[1]), __float_as_uint(t[3]), false, false);
    const u32x2 ov = {pk(__uint_as_float(e0[0]), __uint_as_float(e0[1])), pk(__uint_as_float(e1[0]), __uint_as_float(e1[1]))};
    const int tok = 16 * tt + c.n, col = 16 * ct + 8 * (c.g & 1) + 4 * (c.g >> 1);
    __builtin_amdgcn_raw_buffer_store_b64(ov, mk_rsrc(P.out, 2048 * 640 * 2), (u32)(tok * 640 + col) * 2, 0, O_AUX);
}
DEV void add_a(const Params& P, const Ctx& c, int wk) {
    if (wk >= 5 * A_GRP) { wait_until(now32() + (wk - 5 * A_GRP) * F_STAG); return; }
    const int lane = c.lane, ct = 8 * (wk % 5) + c.wave;
    u32x4 a[24], st[6];
#pragma unroll
    for (int k = 0; k < 24; ++k) a[k] = ld4(c.rs, OFF_WOF + lane * 16, (u32)(ct * 24 + k) * 1024);
    int prog = -1, par = 0;
#pragma unroll
    for (int k = 0; k < 6; ++k) st[k] = ld4(c.rs, OFF_OA + c.tid * 16, (u32)(wk / 5) * 49152u + k * 8192);
    for (int s = wk / 5; s < S_SPLIT; s += A_GRP, par ^= 49152) {
#pragma unroll
        for (int k = 0; k < 6; ++k) *(LAS u32x4*)(c.lds + par + k * 8192 + c.tid * 16) = st[k];
        u32 sp = SPIN_CAP;
        while (prog < (2 * s + 1) / NFB) {
            FENCE();
            int v = (int)__hip_atomic_load(flag(c, F_RB + min(lane, NFB - 1)), RLX_AGENT);
#pragma unroll
            for (int o = 16; o; o >>= 1) v = min(v, __shfl_xor(v, o));
            prog = __builtin_amdgcn_readfirstlane(v);
            if (prog < (2 * s + 1) / NFB) __builtin_amdgcn_s_sleep(16);
            SPIN_TICK(sp);
        }
        f32x4 rb[2]; float gt[2];
#pragma unroll
        for (int tt = 0; tt < 2; ++tt) {
            rb[tt] = __builtin_bit_cast(f32x4, ld4(c.rs, OFF_RB + lane * 16, (u32)((s * 10 + (ct >> 2)) * 8 + tt * 4 + (ct & 3)) * 1024));
            gt[tt] = __uint_as_float(ld1(c.rs, OFF_G + (u32)(16 * tt + c.n) * 4, (u32)s * 128));
        }
#pragma unroll
        for (int k = 0; k < 6; ++k) st[k] = ld4(c.rs, OFF_OA + c.tid * 16, (u32)(s + A_GRP) * 49152u + k * 8192);
        BAR();
        f32x4 d0 = ZERO4, d1 = ZERO4;
#pragma unroll
        for (int k = 0; k < 24; ++k) {
            d0 = mma16(a[k], lds4(c.lds + par + k * 1024 + lane * 16), d0);
            d1 = mma16(a[k], lds4(c.lds + par + (24 + k) * 1024 + lane * 16), d1);
        }
        out_store(P, c, d0 * gt[0] + rb[0], 2 * s, ct);
        out_store(P, c, d1 * gt[1] + rb[1], 2 * s + 1, ct);
    }
}

constexpr int F_BIG = (NSEG - 2 - S_SPLIT) * 10, F_ALL = F_BIG + 20 + 40;
template <int NT, int NC>
DEV void finish_unit(const Params& P, const Ctx& c, int s, int tt0, int ct0, volatile LAS int* uw) {
    const int w = c.wave, n = c.n, lane = c.lane;
    f32x4 res[NT][NC];
    float gt[NT];
#pragma unroll
    for (int tt = 0; tt < NT; ++tt) gt[tt] = __uint_as_float(ld1(c.rs, OFF_G + (u32)(16 * tt + n) * 4, (u32)(s * 2 + tt0) * 64));
#pragma unroll
    for (int i = 0; i < NT * NC; ++i) res[i / NC][i % NC] = ZERO4;
    {
        u32x4 b[3][NT], a[3][NC];
#pragma unroll
        for (int k = 0; k < 3; ++k) {
#pragma unroll
            for (int tt = 0; tt < NT; ++tt) b[k][tt] = ld4(c.rs, OFF_OA + lane * 16, (u32)((s * 2 + tt0 + tt) * 24 + 3 * w + k) * 1024);
#pragma unroll
            for (int ct = 0; ct < NC; ++ct) a[k][ct] = ld4(c.rs, OFF_WOF + lane * 16, (u32)((ct0 + ct) * 24 + 3 * w + k) * 1024);
        }
        SB();
#pragma unroll
        for (int k = 0; k < 3; ++k)
#pragma unroll
            for (int i = 0; i < NT * NC; ++i) res[i / NC][i % NC] = mma16(a[k][i % NC], b[k][i / NC], res[i / NC][i % NC]);
#pragma unroll
        for (int i = 0; i < NT * NC; ++i) res[i / NC][i % NC] *= gt[i / NC];
    }
    u32x4 b[5][NT], a[5][NC];
    wait4<24>(c, F_PT + 4 * w, s - F_AHEAD);
    wait4<8>(c, F_U + 32 * (ct0 / 10) + 4 * w, s);
    wait4<8>(c, F_U + 32 * ((ct0 + NC - 1) / 10) + 4 * w, s);
#pragma unroll
    for (int k = 0; k < 4; ++k)
#pragma unroll
        for (int ct = 0; ct < NC; ++ct) a[k][ct] = ld4(c.rs, OFF_U + (u32)(ct * 32 + 4 * w + k) * 1024 + lane * 16, (u32)(s * 40 + ct0) * 32768u);
    {
        gu32* fp = flag(c, (lane & 4) && s < NSEG - 1 ? F_YR + ct0 + (lane & (NC - 1)) : F_PT + 32 * ((lane >> 2) & 1) + 4 * w + (lane & 3));
        u32 sp = SPIN_CAP;
        while (__builtin_amdgcn_ballot_w64((int)__hip_atomic_load(fp, RLX_AGENT) < s) != 0) { __builtin_amdgcn_s_sleep(NT == 2 ? 6 : 2); SPIN_TICK(sp); }
        FENCE();
    }
#pragma unroll
    for (int k = 0; k < 5; ++k)
#pragma unroll
        for (int tt = 0; tt < NT; ++tt)
            b[k][tt] = k < 4 ? ld4(c.rs, OFF_PT + (u32)(tt * 32 + 4 * w + k) * 1024 + lane * 16, (u32)s * 81920u + (u32)tt0 * 32768u)
                             : ld4(c.rs, OFF_PT + 65536u + (u32)(tt * 8 + w) * 1024 + lane * 16, (u32)s * 81920u + (u32)tt0 * 8192u);
#pragma unroll
    for (int ct = 0; ct < NC; ++ct) a[4][ct] = ld4(c.rs, OFF_YL + (u32)(ct * 8 + w) * 1024 + lane * 16, (u32)(s * 40 + ct0) * 8192u);
    const int drawn = (int)__hip_atomic_load(flag(c, F_FT), RLX_AGENT);
    SB();
    f32x4 acc[NT][NC];
#pragma unroll
    for (int i = 0; i < NT * NC; ++i) acc[i / NC][i % NC] = ZERO4;
#pragma unroll
    for (int k = 0; k < 5; ++k)
#pragma unroll
        for (int i = 0; i < NT * NC; ++i) acc[i / NC][i % NC] = mma16(a[k][i % NC], b[k][i / NC], acc[i / NC][i % NC]);
#pragma unroll
    for (int i = 0; i < NT * NC; ++i)
        *(LAS f32x4*)(c.lds + (w * 8 + i) * 1024 + lane * 16) = res[i / NC][i % NC] + acc[i / NC][i % NC] * (1.0f - gt[i / NC]);
    if (c.tid == 0) uw[1] = drawn;
    BAR();
    if (w < NT * NC) {
        f32x4 t = *(LAS f32x4*)(c.lds + w * 1024 + lane * 16);
#pragma unroll
        for (int i = 1; i < 8; ++i) t += *(LAS f32x4*)(c.lds + (i * 8 + w) * 1024 + lane * 16);
        out_store(P, c, t, 2 * s + tt0 + w / NC, ct0 + w % NC);
    }
}
DEV void finish_units(const Params& P, const Ctx& c) {
    volatile LAS int* uw = (volatile LAS int*)(c.lds + 131072);
    for (;;) {
        if (c.tid == 0) *uw = 1 + (int)__hip_atomic_fetch_add(flag(c, F_FT), 1u, RLX_AGENT);
        BAR();
        const int u = __builtin_amdgcn_readfirstlane(*uw);
        if (u >= F_ALL) break;
        if (u < F_BIG) finish_unit<2, 4>(P, c, S_SPLIT + u / 10, 0, 4 * (u % 10), uw);
        else if (u < F_BIG + 20) finish_unit<1, 4>(P, c, NSEG - 2, (u - F_BIG) & 1, 4 * ((u - F_BIG) >> 1), uw);
        else finish_unit<1, 2>(P, c, NSEG - 1, (u - F_BIG) & 1, 2 * ((u - F_BIG - 20) >> 1), uw);
        if (__builtin_amdgcn_readfirstlane(uw[1]) + 1 >= F_ALL) break;
    }
}

extern "C" __global__ void __launch_bounds__(512, 2) mega_kernel(Params P) {
    extern __shared__ __attribute__((aligned(16))) unsigned char smem[];
    Ctx c;
    c.rs = __builtin_amdgcn_make_buffer_rsrc(P.ws, 0, (int)WS_BYTES, 0x00020000);
    c.rq = (u32x4){(u32)(uintptr_t)P.ws, (u32)((uintptr_t)P.ws >> 32) & 0xFFFFu, WS_BYTES, 0x00020000u};
    c.fl = (gu32*)(P.ws + OFF_FLAG);
    c.tid = threadIdx.x;
    c.wave = __builtin_amdgcn_readfirstlane(threadIdx.x >> 6);
    c.lane = threadIdx.x & 63;
    c.n = c.lane & 15;
    c.g = c.lane >> 4;
    c.lds = (lchar*)smem;
    c.sink = 0;
    const int braw = blockIdx.x, b = braw & 0xFFFF;
    {
        u64 pc; u32 len, keep;
        asm volatile("s_getpc_b64 %0\n.Lmk_pc:\n\ts_mov_b32 %1, .Lmk_tail-.Lmk_pc" : "=s"(pc), "=s"(len));
        const u32 off = (u32)((b >> 3) * 64 + c.lane) * 64;
        if (c.wave == 7 && off < len)
            asm volatile("s_mov_b32 %0, m0\n\ts_mov_b32 m0, %3\n\ts_nop 0\n\tglobal_load_lds_dword %1, %2\n\ts_mov_b32 m0, %0"
                         : "=&s"(keep) : "v"(off), "s"(pc), "s"(LDS_BYTES - 4096) : "memory");
    }
    const int xcc = (int)(__builtin_amdgcn_s_getreg(20 | (3 << 11)) & 7u);
    const int rep = xcc & 3;
    c.b = b;
    c.r8 = braw >> 16 ? (braw >> 16) & 7 : (xcc - b) & 7;
    if (b == 0 && c.wave == 0) __hip_atomic_store((gu64*)(P.ws + OFF_EPOCH + 128), (launch_tag(P) << 3) | (u64)c.r8, RLX_AGENT);
    const int x8 = (b + c.r8) & 7, idx = (b >> 3) * 4 + (x8 & 3), ro = idx + (x8 & 4) * 32;
    if (ro < 32) { if (c.wave < 4) role_q(P, c, ro, rep); else role_k(P, c, ro, rep); DUMP(ro); }
    else if (ro < 64) { role_vom(P, c, ro - 32, rep); DUMP(ro); }
    else if (ro < 104) { role_red(P, c, ro - 64, rep); DUMP(ro); }
    else if (ro < 120 || ro >= 240) role_px(P, c, ro < 120 ? ro - 104 : ro - 240, ro < 120);
    else if (ro < 128) {
        if (HELP) for (int i = 0; i < 2; ++i) warm_q(P, c, (ro & 7) + 8 * i);
        asm volatile("" :: "v"(c.sink));
        check_role(P, c);
        wait_armed(P, c);
        BAR();
        if (c.tid == 0) __hip_atomic_fetch_add(flag(c, F_BAR3), 1u, RLX_AGENT);
    }
    else if (ro < 160) { role_vox(P, c, ro - 128); DUMP(ro + 96); }
    else
    {
        {
            const int wk = idx - 32;
            WSTAMP(0);
            if (HELP && wk >= NATT) { warm_head(P, c, wk - NATT, 0, 1); warm_head(P, c, wk - NATT + 16, 0, 1); }
            asm volatile("" :: "v"(c.sink));
            if (wk < NATT) {
                wrk_qkv(P, c, wk);
                WSTAMP(1);
                wait_armed(P, c);
                wrk_barrier(c, F_BAR1, NATT, true);
                WSTAMP(2);
                wrk_attn(P, c, wk, flag(c, F_TK + wk));
                WSTAMP(3);
                wrk_barrier(c, F_BAR2, NATT, true);
                wrk_barrier(c, F_BAR3, NFB + 8, false);
                WSTAMP(4);
                add_a(P, c, wk);
            } else {
                side_jobs(P, c, (wk - NATT) * 8 + c.wave);
                check_role(P, c);
                WSTAMP(1);
                wait_armed(P, c);
                wrk_barrier(c, F_BAR3, NFB + 8, true);
                WSTAMP(2);
                sum_b(c, wk - NATT);
                WSTAMP(3);
                wrk_barrier(c, F_BAR2, NATT, false);
                WSTAMP(4);
            }
            WSTAMP(6);
        }
    }
    c.lane = (int)__builtin_amdgcn_mbcnt_hi(~0u, __builtin_amdgcn_mbcnt_lo(~0u, 0u));
    PIN("+v"(c.lane));
    c.n = c.lane & 15; c.g = c.lane >> 4; c.tid = c.wave * 64 + c.lane;
    if (ro >= 128) {
        if (ro < 160 || ro >= 240) {
            __builtin_amdgcn_s_setprio(0);
            wrk_barrier(c, F_BAR2, NATT, false);
            wrk_barrier(c, F_BAR3, NFB + 8, false);
        }
        finish_units(P, c);
        if (ro == 160 && c.tid == 0) __hip_atomic_store((gu64*)(P.ws + OFF_EPOCH), launch_tag(P), RLX_AGENT);
        const int wk = ro >= 160 && ro < 240 ? ro - 160 : 87; (void)wk;
        WSTAMP(5);
    }
}

extern "C" void kernel_launch(void* const* d_in, const int* in_sizes, int n_in,
                              void* d_out, int out_size, void* d_ws, size_t ws_size,
                              hipStream_t stream) {
    (void)in_sizes; (void)n_in; (void)out_size; (void)ws_size;
    Params P;
    P.x = (const u16*)d_in[0];
    P.wqkv = (const u16*)d_in[1];
    P.wo = (const u16*)d_in[2];
    P.mem0 = (const u16*)d_in[4];
    P.Wq = (const u16*)d_in[5];
    P.Wk = (const u16*)d_in[6];
    P.Wv = (const u16*)d_in[7];
    P.Wo = (const u16*)d_in[8];
    P.gw = (const u16*)d_in[10];
    P.out = (u16*)d_out;
    P.ws = (unsigned char*)(((uintptr_t)d_ws + (2u * MB - 1)) & ~(uintptr_t)(2u * MB - 1));
    (void)hipFuncSetAttribute((const void*)mega_kernel, hipFuncAttributeMaxDynamicSharedMemorySize, LDS_BYTES);
    mega_kernel<<<256, 512, LDS_BYTES, stream>>>(P);
}
```
